```python
import torch

def _softmax(x, axis=-1):
    return torch.softmax(x, dim=axis)
def _gelu(x):
    return torch.nn.functional.gelu(x, approximate="tanh")
def _silu(x):
    return torch.nn.functional.silu(x)
def _sigmoid(x):
    return torch.sigmoid(x)
def _layernorm(x, axis=-1, eps=1e-5):
    m = x.mean(dim=axis, keepdim=True)
    v = x.var(dim=axis, keepdim=True, unbiased=False)
    return (x - m) / torch.sqrt(v + eps)
def _scatter_add(tgt, idx, src, axis=0):
    return tgt.clone().index_add_(axis, idx, src)
def _segment_sum(src, seg, n):
    out = torch.zeros((n,) + src.shape[1:], dtype=src.dtype)
    return out.index_add_(0, seg, src)
def _pad(x, pw):
    flat = tuple(v for pair in reversed(pw) for v in pair)
    return torch.nn.functional.pad(x, flat)

kernel_name = 'parallel_sum__gqa_block_cached__moe_arch_mod__decode'

def setup_inputs():

    _w = (lambda a: (lambda t: t.to(torch.bfloat16) if t.is_floating_point() else t)(torch.as_tensor(a)))
    def _A_setup():
        B, Sq, D = 1, 1, 512
        Hq, Hkv, Sk, Dh = 32, 8, 65536, 128
        assert Hq % Hkv == 0, (Hq, Hkv)
        return {
            "x": _w((lambda *s: torch.randn(*s, dtype=torch.bfloat16) * 0.05)(B, Sq, D)),
            "k_cache": _w((lambda *s: torch.randn(*s, dtype=torch.bfloat16) * 3.2)(B, Hkv, Sk, Dh)),
            "v_cache": _w((lambda *s: torch.randn(*s, dtype=torch.bfloat16) * 1.6)(B, Hkv, Sk, Dh)),
            "wq": _w((lambda *s: torch.randn(*s, dtype=torch.bfloat16) * 3.2)(D, Hq * Dh) * (D ** -0.5)),
            "wo": _w((lambda *s: torch.randn(*s, dtype=torch.bfloat16) * 1.6)(Hq * Dh, D) * ((Hq * Dh) ** -0.5)),
        }
    _A_in = _A_setup()

    def _B_setup():
        B, S, D, F, C = 1, 1, 512, 1792, 1
        return {
            "x": _w((lambda *s: torch.randn(*s, dtype=torch.bfloat16) * 0.05)(B, S, D)),
            "w_r": _w((lambda *s: torch.randn(*s, dtype=torch.bfloat16) * 0.1)(D)),
            "w_gate": _w((lambda *s: torch.randn(*s, dtype=torch.bfloat16) * 0.1)(D, F)),
            "w_up": _w((lambda *s: torch.randn(*s, dtype=torch.bfloat16) * 0.1)(D, F)),
            "w_down": _w((lambda *s: torch.randn(*s, dtype=torch.bfloat16) * 0.1)(F, D)),
        }
    _B_in = _B_setup()

    return {
        "A_x": _A_in["x"],
        "A_k_cache": _A_in["k_cache"],
        "A_v_cache": _A_in["v_cache"],
        "A_wq": _A_in["wq"],
        "A_wo": _A_in["wo"],
        "B_x": _B_in["x"],
        "B_w_r": _B_in["w_r"],
        "B_w_gate": _B_in["w_gate"],
        "B_w_up": _B_in["w_up"],
        "B_w_down": _B_in["w_down"],
    }

def reference(A_x, A_k_cache, A_v_cache, A_wq, A_wo, B_x, B_w_r, B_w_gate, B_w_up, B_w_down):
    def _A_ref(x, k_cache, v_cache, wq, wo):
        B, Sq, D = x.shape
        Hq, Dh = 32, 128
        Hkv = k_cache.shape[1]
        rep = Hq // Hkv
        q = (lambda x, *d: x.permute(*d))((x @ wq).reshape(B, Sq, Hq, Dh), 0, 2, 1, 3)
        k = (lambda x, n, axis: torch.repeat_interleave(x, n, dim=axis))(k_cache, rep, axis=1)
        v = (lambda x, n, axis: torch.repeat_interleave(x, n, dim=axis))(v_cache, rep, axis=1)
        scores = torch.einsum("bhqd,bhkd->bhqk", q, k) * (Dh ** -0.5)
        p = _softmax(scores, axis=-1)
        o = torch.einsum("bhqk,bhkd->bhqd", p, v)
        return (lambda x, *d: x.permute(*d))(o, 0, 2, 1, 3).reshape(B, Sq, Hq * Dh) @ wo

    def _B_ref(x, w_r, w_gate, w_up, w_down):
        B, S, D = x.shape
        C = 1
        r = x @ w_r
        idx = torch.argsort(-r, axis=-1)[:, :C]
        idx_e = (lambda x, n, axis: torch.repeat_interleave(x, n, dim=axis))(idx.reshape(B, C, 1), D, axis=2)
        x_sel = (lambda x, i, axis: torch.take_along_dim(x, i, dim=axis))(x, idx_e, axis=1)
        g = _silu(x_sel @ w_gate)
        u = x_sel @ w_up
        y = (g * u) @ w_down
        r_sel = _sigmoid((lambda x, i, axis: torch.take_along_dim(x, i, dim=axis))(r, idx, axis=1))
        y = y * r_sel.reshape(B, C, 1)
        b_off = (torch.arange(B) * S).reshape(B, 1)
        flat_idx = (idx + b_off).reshape(B * C)
        zero = (x * 0.0).reshape(B * S, D)
        upd = _scatter_add(zero, flat_idx, y.reshape(B * C, D), axis=0)
        return x + upd.reshape(B, S, D)

    x = A_x
    xn = _layernorm(x, axis=-1)
    return x + _A_ref(xn, A_k_cache, A_v_cache, A_wq, A_wo) + _B_ref(xn, B_w_r, B_w_gate, B_w_up, B_w_down)
```

```cpp
#define OSTORE 0
#include <hip/hip_runtime.h>
#include <hip/hip_bf16.h>
#include <stdint.h>

#define GAS __attribute__((address_space(1)))
#define LAS __attribute__((address_space(3)))
typedef unsigned v4u __attribute__((ext_vector_type(4)));
typedef unsigned v2u __attribute__((ext_vector_type(2)));
typedef float f4 __attribute__((ext_vector_type(4)));
typedef float f2 __attribute__((ext_vector_type(2)));
typedef short s16x4 __attribute__((ext_vector_type(4)));
typedef __bf16 bf16x8 __attribute__((ext_vector_type(8)));
typedef __bf16 bf16x2 __attribute__((ext_vector_type(2)));

#define END_MARK fused_decode_code_end
#define KERNEL_NAME fused_decode_kernel
#define STR2(x) #x
#define STR(x) STR2(x)
#define NSTEP_RUN NSTEP
#define HOLDT 0
#define WSTAIL 1
#define ADAPT 1
#define PACE0 4
#define KP 6
#define UNOM 576
#define NEXCH 8
#define XSTOP 208
#define PUBM 3
#define CSLEEP 10
#define TAU16 218
#define D1 18
#define D2 10
#define DY 10
#define T1 0
#define T2 0
#define GPROG 240

namespace {

constexpr int D = 512, DH = 128, SK = 65536, NQ = 4096, FF = 1792;
constexpr int NWG = 256, NTHR = 448;
constexpr int KEYS_PER_WG = SK / 32;
constexpr int NSTEP = KEYS_PER_WG / 32;
constexpr int RING = 4;
constexpr int NFFN = FF / 64;
constexpr float SCALE = 0.08838834764831845f, LOG2E = 1.4426950408889634f, FMIN = -3.4028234664e38f;
constexpr unsigned SPIN_CAP = 1u << 16;

constexpr int ROW = 4096 + 128;
constexpr int WS_EPOCH = 0;
constexpr int WS_Q = 4096;
constexpr int WS_GU = WS_Q + 8 * 4 * 4096;
constexpr int WS_Y = WS_GU + NFFN * 8192;
constexpr int WS_O = WS_Y + NFFN * ROW;
constexpr int WS_A = WS_O + 256 * ROW;
constexpr int WS_PL = WS_A + 256 * ROW;
constexpr int WS_END = WS_PL + 256 * 128;
constexpr size_t WS_SPAN = 4u << 20;
static_assert(WS_END + 65536 <= WS_SPAN, "workspace map");

constexpr int L_Q = 0, L_E = 1024, L_FLAG = 1032, L_CNT = 1036, L_PROG = 1040, L_LVL = 1056, L_TEND = 1060, L_B = 1072, L_FIN = 1136;
constexpr int L_ML = 1280;
constexpr int REC = 4 * 132 * 4, L_REC = 1536;
constexpr int L_WO = 16384, L_WG = 32768, L_WU = 40960, L_WD = 49152;
constexpr int LDS_BYTES = 84 * 1024;

struct Params {
  const unsigned short *x, *k, *v, *wq, *wo, *wr, *wg, *wu, *wd;
  unsigned* out;
  unsigned char* ws;
};

__device__ __forceinline__ float asf(unsigned u) { return __builtin_bit_cast(float, u); }
__device__ __forceinline__ unsigned asu(float f) { return __builtin_bit_cast(unsigned, f); }
__device__ __forceinline__ float lo(unsigned u) { return asf(u << 16); }
__device__ __forceinline__ float hi(unsigned u) { return asf(u & 0xffff0000u); }
__device__ __forceinline__ unsigned pk(float a, float b) {
  bf16x2 r = {(__bf16)a, (__bf16)b};
  return __builtin_bit_cast(unsigned, r);
}
__device__ __forceinline__ float rbf(float a) { return lo(pk(a, a)); }
__device__ __forceinline__ float rcp(float a) { return __builtin_amdgcn_rcpf(a); }
__device__ __forceinline__ float ex2(float a) { return __builtin_amdgcn_exp2f(a); }
__device__ __forceinline__ unsigned xor3(unsigned a, unsigned b, unsigned c) { return __builtin_amdgcn_bitop3_b32(a, b, c, 0x96); }
__device__ __forceinline__ unsigned or3(unsigned a, unsigned b, unsigned c) { return __builtin_amdgcn_bitop3_b32(a, b, c, 0xFE); }
__device__ __forceinline__ unsigned uni(unsigned a) { return (unsigned)__builtin_amdgcn_readfirstlane((int)a); }
__device__ __forceinline__ unsigned clock32() { return (unsigned)__builtin_amdgcn_s_memrealtime(); }
__device__ __forceinline__ float dot2(unsigned a, unsigned b, float c) {
  return __builtin_amdgcn_fdot2_f32_bf16(__builtin_bit_cast(bf16x2, a), __builtin_bit_cast(bf16x2, b), c, false);
}
template <class T> __device__ __forceinline__ void pin(T& x) { asm volatile("" : "+v"(x)); }
template <int CTRL> __device__ __forceinline__ float dpp(float x) {
  return asf(__builtin_amdgcn_mov_dpp(__builtin_bit_cast(int, x), CTRL, 0xf, 0xf, true));
}
constexpr int X1 = 0xB1, X2 = 0x4E, X7 = 0x141, R4 = 0x124, R8 = 0x128;
__device__ __forceinline__ float x32_tr(float a, float b) {
  auto s = __builtin_amdgcn_permlane32_swap(asu(a), asu(b), false, false);
  return asf(s[0]) + asf(s[1]);
}
__device__ __forceinline__ float x16_tr(float a, float b) {
  auto s = __builtin_amdgcn_permlane16_swap(asu(a), asu(b), false, false);
  return asf(s[0]) + asf(s[1]);
}
__device__ __forceinline__ float rows_add(float x) { x = x16_tr(x, x); return x32_tr(x, x); }
__device__ __forceinline__ float rows_max(float x) {
  auto s = __builtin_amdgcn_permlane16_swap(asu(x), asu(x), false, false);
  x = fmaxf(asf(s[0]), asf(s[1]));
  auto t = __builtin_amdgcn_permlane32_swap(asu(x), asu(x), false, false);
  return fmaxf(asf(t[0]), asf(t[1]));
}
__device__ __forceinline__ float wave_sum(float x) {
  x += dpp<X1>(x); x += dpp<X2>(x); x += dpp<X7>(x); x += dpp<R8>(x);
  return rows_add(x);
}
__device__ __forceinline__ void lds_barrier() {
  asm volatile("s_waitcnt lgkmcnt(0)" ::: "memory");
  __builtin_amdgcn_s_barrier();
  asm volatile("" ::: "memory");
}
template <int N> __device__ __forceinline__ void pace() {
  __builtin_amdgcn_sched_barrier(0);
  asm volatile("s_waitcnt vmcnt(%0)" :: "i"(N) : "memory");
  __builtin_amdgcn_sched_barrier(0);
}
__device__ __forceinline__ void pace_lvl(unsigned lvl) {
  __builtin_amdgcn_sched_barrier(0);
  asm volatile("s_cmp_gt_u32 %0, 3\n\ts_cbranch_scc1 4f\n\ts_cmp_gt_u32 %0, 1\n\ts_cbranch_scc1 2f\n\t"
               "s_cmp_eq_u32 %0, 1\n\ts_cbranch_scc1 1f\n\ts_waitcnt vmcnt(2)\n1:\n\ts_waitcnt vmcnt(3)\n\ts_branch 9f\n"
               "2:\n\ts_cmp_eq_u32 %0, 3\n\ts_cbranch_scc1 3f\n\ts_waitcnt vmcnt(4)\n3:\n\ts_waitcnt vmcnt(5)\n\ts_branch 9f\n"
               "4:\n\ts_cmp_gt_u32 %0, 5\n\ts_cbranch_scc1 6f\n\t"
               "s_cmp_eq_u32 %0, 5\n\ts_cbranch_scc1 5f\n\ts_waitcnt vmcnt(6)\n5:\n\ts_waitcnt vmcnt(7)\n\ts_branch 9f\n"
               "6:\n\ts_cmp_gt_u32 %0, 7\n\ts_cbranch_scc1 9f\n\t"
               "s_cmp_eq_u32 %0, 7\n\ts_cbranch_scc1 7f\n\ts_waitcnt vmcnt(8)\n7:\n\ts_waitcnt vmcnt(10)\n9:"
               :: "s"(lvl) : "scc");
  __builtin_amdgcn_sched_barrier(0);
}
constexpr int SC1 = 16, NT = 2;
__device__ __forceinline__ void put2(__amdgpu_buffer_rsrc_t rs, int off, unsigned a, unsigned b, v2u e) {
  __builtin_amdgcn_raw_buffer_store_b128(v4u{a, b, a ^ e.x, b ^ e.y}, rs, off, 0, SC1);
}
__device__ __forceinline__ void put3(__amdgpu_buffer_rsrc_t rs, int off, unsigned a, unsigned b, unsigned c, v2u e) {
  __builtin_amdgcn_raw_buffer_store_b128(v4u{a, b, c, xor3(a, b, c) ^ e.x}, rs, off, 0, SC1);
}
template <int N, bool THREE> __device__ __forceinline__ void gather(__amdgpu_buffer_rsrc_t rs, const int (&off)[N], v2u e, v4u (&g)[N]) {
#pragma unroll 1
  for (unsigned spin = 0; spin < SPIN_CAP; ++spin) {
    asm volatile("" ::: "memory");
#pragma unroll
    for (int i = 0; i < N; ++i) g[i] = __builtin_amdgcn_raw_buffer_load_b128(rs, off[i], 0, SC1);
    unsigned bad = 0;
#pragma unroll
    for (int i = 0; i < N; ++i) {
      if (THREE) bad |= xor3(xor3(g[i].x, g[i].y, g[i].z), g[i].w, e.x);
      else bad = or3(xor3(g[i].x, g[i].z, e.x), xor3(g[i].y, g[i].w, e.y), bad);
    }
    if (__builtin_amdgcn_ballot_w64(bad != 0u) == 0ull) break;
  }
}
template <unsigned V> __device__ __forceinline__ void wait_word(LAS unsigned char* w) {
#pragma unroll 1
  for (unsigned spin = 0; spin < (1u << 22); ++spin) {
    if (uni(*(volatile LAS unsigned*)w) >= V) break;
    __builtin_amdgcn_s_sleep(1);
  }
  asm volatile("" ::: "memory");
}
__device__ __forceinline__ unsigned progress(LAS unsigned char* L) {
  const v4u pr = *(volatile LAS v4u*)(L + L_PROG);
  return uni(pr.x + pr.y + pr.z + pr.w);
}
__device__ __forceinline__ void wait_until(unsigned t) {
#pragma unroll 1
  for (int i = 0; i < 128 && (int)(clock32() - t) < 0; ++i) {}
}
__device__ __forceinline__ void dma(const void* src, unsigned dst) {
  unsigned keep;
  asm volatile("s_mov_b32 %0, m0\n\ts_mov_b32 m0, %2\n\ts_nop 0\n\tglobal_load_lds_dwordx4 %1, off nt\n\ts_mov_b32 m0, %0"
               : "=&s"(keep) : "v"(src), "s"(dst) : "memory");
}
__device__ __forceinline__ void ln_stats(v4u xa, float& mean, float& rden) {
  const unsigned one = 0x3f803f80u;
  const float mu = wave_sum(dot2(xa.x, one, dot2(xa.y, one, 0.f)) + dot2(xa.z, one, dot2(xa.w, one, 0.f))) * (1.0f / D);
  float q0 = 0.f, q1 = 0.f;
#pragma unroll
  for (int j = 0; j < 4; ++j) { const float a = lo(xa[j]) - mu, b = hi(xa[j]) - mu; q0 = fmaf(a, a, q0); q1 = fmaf(b, b, q1); }
  const float var = rbf(wave_sum(q0 + q1) * (1.0f / D));
  mean = rbf(mu);
  rden = rcp(rbf(__builtin_amdgcn_sqrtf(rbf(var + 1e-5f))));
}
__device__ __forceinline__ unsigned ln_pair(unsigned xx, float mean, float rden) {
  const unsigned d = pk(lo(xx) - mean, hi(xx) - mean);
  return pk(lo(d) * rden, hi(d) * rden);
}
__device__ __forceinline__ v4u ln_apply(v4u xa, float mean, float rden) {
  return v4u{ln_pair(xa.x, mean, rden), ln_pair(xa.y, mean, rden), ln_pair(xa.z, mean, rden), ln_pair(xa.w, mean, rden)};
}
struct Pairs { unsigned c[8]; };
__device__ __forceinline__ Pairs pair_rows(v4u w0, v4u w1) {
  Pairs r;
#pragma unroll
  for (int k = 0; k < 4; ++k) {
    r.c[2 * k] = __builtin_amdgcn_perm(w1[k], w0[k], 0x05040100u);
    r.c[2 * k + 1] = __builtin_amdgcn_perm(w1[k], w0[k], 0x07060302u);
  }
  return r;
}
__device__ __forceinline__ void mac(const Pairs& w, unsigned x, float (&acc)[8]) {
#pragma unroll
  for (int k = 0; k < 8; ++k) acc[k] = dot2(w.c[k], x, acc[k]);
}
__device__ __forceinline__ f2 reduce8(const float (&acc)[8]) {
  const float t0 = x32_tr(acc[0], acc[4]), t1 = x32_tr(acc[1], acc[5]), t2 = x32_tr(acc[2], acc[6]), t3 = x32_tr(acc[3], acc[7]);
  const float u0 = x16_tr(t0, t2), u1 = x16_tr(t1, t3);
  return f2{u0 + dpp<R8>(u0), u1 + dpp<R8>(u1)};
}
__device__ __forceinline__ float reduce_quads(float a, float b) {
  float t = x32_tr(a, b);
  t = x16_tr(t, t);
  t += dpp<R4>(t);
  return t + dpp<R8>(t);
}
#define STAMP(i) do {} while (0)
#define FLUSH(a, b) do {} while (0)

struct Acc { f4 o0, o1; float m, l; };

__device__ __forceinline__ unsigned scores_to_p(v4u ka, v4u kb, bf16x8 qf, Acc& s) {
  const f4 z = {0.f, 0.f, 0.f, 0.f};
  const f4 da = __builtin_amdgcn_mfma_f32_16x16x32_bf16(__builtin_bit_cast(bf16x8, ka), qf, z, 0, 0, 0);
  const f4 db = __builtin_amdgcn_mfma_f32_16x16x32_bf16(__builtin_bit_cast(bf16x8, kb), qf, z, 0, 0, 0);
  float sa = (dpp<0x00>(da[0]) + dpp<0x55>(da[1])) + (dpp<0xAA>(da[2]) + dpp<0xFF>(da[3]));
  float sb = (dpp<0x00>(db[0]) + dpp<0x55>(db[1])) + (dpp<0xAA>(db[2]) + dpp<0xFF>(db[3]));
  unsigned u = pk(sa, sb);
  u = pk(lo(u) * SCALE, hi(u) * SCALE);
  sa = lo(u); sb = hi(u);
  float mx = fmaxf(sa, sb);
  if (__builtin_amdgcn_ballot_w64(mx > s.m) != 0ull) {
    mx = fmaxf(s.m, rows_max(mx));
    const float al = ex2((s.m - mx) * LOG2E);
    s.o0 *= al; s.o1 *= al; s.l *= al; s.m = mx;
  }
  const float pa = ex2((sa - s.m) * LOG2E), pb = ex2((sb - s.m) * LOG2E);
  s.l += pa + pb;
  return pk(pa, pb);
}
__device__ __forceinline__ void pv(v4u va, v4u vb, unsigned pp, v4u sel, Acc& s) {
  const v2u ba = {__builtin_amdgcn_perm(pp, pp, sel.x), __builtin_amdgcn_perm(pp, pp, sel.y)};
  const v2u bb = {__builtin_amdgcn_perm(pp, pp, sel.z), __builtin_amdgcn_perm(pp, pp, sel.w)};
  s.o0 = __builtin_amdgcn_mfma_f32_16x16x16bf16_1k(__builtin_bit_cast(s16x4, v2u{va.x, va.y}), __builtin_bit_cast(s16x4, ba), s.o0, 0, 0, 0);
  s.o1 = __builtin_amdgcn_mfma_f32_16x16x16bf16_1k(__builtin_bit_cast(s16x4, v2u{va.z, va.w}), __builtin_bit_cast(s16x4, ba), s.o1, 0, 0, 0);
  s.o0 = __builtin_amdgcn_mfma_f32_16x16x16bf16_1k(__builtin_bit_cast(s16x4, v2u{vb.x, vb.y}), __builtin_bit_cast(s16x4, bb), s.o0, 0, 0, 0);
  s.o1 = __builtin_amdgcn_mfma_f32_16x16x16bf16_1k(__builtin_bit_cast(s16x4, v2u{vb.z, vb.w}), __builtin_bit_cast(s16x4, bb), s.o1, 0, 0, 0);
}

}

extern "C" __global__ void __launch_bounds__(NTHR, 2) KERNEL_NAME(Params p) {
  __shared__ __attribute__((aligned(16))) unsigned char smem[LDS_BYTES];
  LAS unsigned char* L = (LAS unsigned char*)smem;
  unsigned long long code;
  int code_len;
  asm volatile("s_getpc_b64 %0\n\ts_mov_b32 %1, " STR(END_MARK) "@rel32@lo+4" : "=s"(code), "=s"(code_len));
  const int lane = threadIdx.x & 63, wave = __builtin_amdgcn_readfirstlane(threadIdx.x >> 6);
  const int b = blockIdx.x, g = b & 7, s = b >> 3;
  const int gx = g ^ 1;
  const __amdgpu_buffer_rsrc_t rs = __builtin_amdgcn_make_buffer_rsrc((void*)p.ws, 0, WS_END, 0x00020000);

  if (wave < 4) {
    STAMP(0);
    const unsigned hold0 = clock32();
    const size_t base = ((size_t)g * SK + (size_t)s * KEYS_PER_WG) * DH;
    const __amdgpu_buffer_rsrc_t rk = __builtin_amdgcn_make_buffer_rsrc((void*)(p.k + base), 0, KEYS_PER_WG * DH * 2, 0x00020000);
    const __amdgpu_buffer_rsrc_t rv = __builtin_amdgcn_make_buffer_rsrc((void*)(p.v + base), 0, KEYS_PER_WG * DH * 2, 0x00020000);
    const int h = (lane >> 2) & 3, c = lane & 3;
    const int frag = h * 256 + (lane >> 4) * 64 + c * 16;
    int ko = wave * 2048 + frag, vo = wave * 2048 + lane * 16;
    constexpr int STRIDE = 32 * DH * 2;
    if (threadIdx.x == 0) {
      *(volatile LAS v2u*)(L + L_FLAG) = v2u{0u, 0u};
      *(volatile LAS v4u*)(L + L_PROG) = v4u{0u, 0u, 0u, 0u};
      *(volatile LAS v2u*)(L + L_LVL) = v2u{2u, 0u};
    }
    lds_barrier();
    {
      const unsigned lb = (unsigned)(size_t)L;
      const unsigned short* wo = p.wo + (size_t)(gx * 512 + s * 16 + 4 * wave) * D + 8 * lane;
#pragma unroll
      for (int i = 0; i < 4; ++i) dma(wo + i * D, lb + L_WO + (4 * wave + i) * 1024);
      if (s < NFFN) {
        const int row = 8 * (lane >> 3) + 2 * wave;
        const size_t gu = (size_t)(g * 64 + row) * FF + s * 64 + (lane & 7) * 8;
        const unsigned short* wd = p.wd + (size_t)(s * 64 + row) * D + g * 64 + (lane & 7) * 8;
        dma(p.wg + gu, lb + L_WG + 2 * wave * 1024);
        dma(p.wu + gu, lb + L_WU + 2 * wave * 1024);
        pace<PACE0>();
        dma(p.wg + gu + FF, lb + L_WG + (2 * wave + 1) * 1024);
        dma(p.wu + gu + FF, lb + L_WU + (2 * wave + 1) * 1024);
        pace<PACE0>();
        dma(wd, lb + L_WD + 2 * wave * 1024);
        dma(wd + D, lb + L_WD + (2 * wave + 1) * 1024);
      }
    }
    v4u ka[RING], kb[RING], va[RING], vb[RING];
#pragma unroll
    for (int r = 0; r < RING; ++r) {
      pace<PACE0>();
      if (r == (PACE0 + 3) / 4 && lane == 0)
        __hip_atomic_fetch_add((LAS unsigned*)(L + L_CNT), 1u, __ATOMIC_RELAXED, __HIP_MEMORY_SCOPE_WORKGROUP);
      ka[r] = __builtin_amdgcn_raw_buffer_load_b128(rk, ko, 0, NT);
      kb[r] = __builtin_amdgcn_raw_buffer_load_b128(rk, ko + 1024, 0, NT);
      pace<PACE0>();
      va[r] = __builtin_amdgcn_raw_buffer_load_b128(rv, vo, 0, NT);
      vb[r] = __builtin_amdgcn_raw_buffer_load_b128(rv, vo + 1024, 0, NT);
      ko += STRIDE; vo += STRIDE;
    }
    __builtin_amdgcn_sched_barrier(0);
    const unsigned z = 0x0c0c0c0cu;
    const v4u sel = {c == 0 ? 0x0c0c0100u : c == 1 ? 0x01000c0cu : z, c == 2 ? 0x0c0c0100u : c == 3 ? 0x01000c0cu : z,
                     c == 0 ? 0x0c0c0302u : c == 1 ? 0x03020c0cu : z, c == 2 ? 0x0c0c0302u : c == 3 ? 0x03020c0cu : z};
    const int id = s * 8 + g;
    int a_ml = L_ML + h * 32 + wave * 8, a_ow = L_REC + wave * REC + (h * 132 + (lane >> 4) * 32 + c) * 4, a_or = L_REC + (wave * 132 + 2 * lane) * 4;
    int a_po = WS_O + (g * 32 + s) * ROW + threadIdx.x * 16;
    pin(a_ml); pin(a_ow); pin(a_or); pin(a_po);
    STAMP(14);
    wait_word<1>(L + L_FLAG);
    const bf16x8 qf = __builtin_bit_cast(bf16x8, *(const LAS v4u*)(L + L_Q + frag));
    v2u e = *(const LAS v2u*)(L + L_E);
    pin(e);
    STAMP(1);
    Acc a;
    a.o0 = f4{0.f, 0.f, 0.f, 0.f}; a.o1 = a.o0; a.m = FMIN; a.l = 0.f;
#pragma unroll 1
    for (int i = NSTEP_RUN - RING; i >= 0; i -= RING) {
#pragma unroll
      for (int r = 0; r < RING; ++r) {
        unsigned lv = *(volatile LAS unsigned*)(L + L_LVL);
        *(volatile LAS unsigned*)(L + L_PROG + 4 * wave) = (unsigned)(NSTEP - RING + r - i);
        const unsigned pp = scores_to_p(ka[r], kb[r], qf, a);
        lv = i ? uni(lv) : 8u;
        pace_lvl(lv);
        ka[r] = __builtin_amdgcn_raw_buffer_load_b128(rk, ko, 0, NT);
        kb[r] = __builtin_amdgcn_raw_buffer_load_b128(rk, ko + 1024, 0, NT);
        if (r == RING - 1) *(volatile LAS f2*)(L + a_ml) = f2{a.m, rows_add(a.l)};
        __builtin_amdgcn_sched_barrier(0);
        pv(va[r], vb[r], pp, sel, a);
        pace_lvl(lv);
        va[r] = __builtin_amdgcn_raw_buffer_load_b128(rv, vo, 0, NT);
        vb[r] = __builtin_amdgcn_raw_buffer_load_b128(rv, vo + 1024, 0, NT);
        ko += STRIDE; vo += STRIDE;
        __builtin_amdgcn_sched_barrier(0);
      }
    }
    STAMP(2);
#pragma unroll
    for (int i = 0; i < 4; ++i) { *(LAS float*)(L + a_ow + 32 * i) = a.o0[i]; *(LAS float*)(L + a_ow + 32 * i + 16) = a.o1[i]; }
    lds_barrier();
    {
      const f4 ml0 = *(const LAS f4*)(L + L_ML + wave * 32), ml1 = *(const LAS f4*)(L + L_ML + wave * 32 + 16);
      const f2 t0 = *(const LAS f2*)(L + a_or), t1 = *(const LAS f2*)(L + a_or + REC), t2 = *(const LAS f2*)(L + a_or + 2 * REC), t3 = *(const LAS f2*)(L + a_or + 3 * REC);
      __builtin_amdgcn_sched_barrier(0);
      const float M = fmaxf(fmaxf(ml0.x, ml0.z), fmaxf(ml1.x, ml1.z));
      const float f0 = ex2((ml0.x - M) * LOG2E), f1 = ex2((ml0.z - M) * LOG2E), f2_ = ex2((ml1.x - M) * LOG2E), f3 = ex2((ml1.z - M) * LOG2E);
      const f2 o = (f0 * t0 + f1 * t1) + (f2_ * t2 + f3 * t3);
      const float ls = fmaf(f0, ml0.y, f1 * ml0.w) + fmaf(f2_, ml1.y, f3 * ml1.w);
      put3(rs, a_po, asu(o.x), asu(o.y), asu((lane & 1) ? ls : M), e);
    }
    STAMP(3);
    if (wave == 0) { FLUSH(0, 3); FLUSH(14, 14); }
    if (s >= 8) return;
    int off[4];
#pragma unroll
    for (int i = 0; i < 4; ++i) { off[i] = WS_A + (64 * wave + 16 * i + (lane >> 2)) * ROW + (4 * id + c) * 16; pin(off[i]); }
    GAS unsigned short* po = (GAS unsigned short*)p.out + 2 * (16 * (id & 15) + 4 * c + (id >> 4)) + ((lane >> 2) & 1);
    int a_fw = L_FIN + wave * 32 + (lane >> 5) * 16 + c * 4;
    pin(po); pin(a_fw);
    const unsigned t_end = uni(*(volatile LAS unsigned*)(L + L_TEND));
    wait_word<2>(L + L_FLAG);
    if (D2) __builtin_amdgcn_s_sleep(D2);
    if (T2) wait_until(t_end + T2);
    v4u t[4];
    gather<4, false>(rs, off, e, t);
    STAMP(10);
    const float av = reduce_quads((asf(t[0].x) + asf(t[1].x)) + (asf(t[2].x) + asf(t[3].x)), (asf(t[0].y) + asf(t[1].y)) + (asf(t[2].y) + asf(t[3].y)));
    if ((lane & 28) == 0) *(LAS float*)(L + a_fw) = av;
    lds_barrier();
    if (wave == 0 && lane < 8) {
      const LAS float* fin = (const LAS float*)(L + L_FIN) + lane;
      const f2 bx = *(const LAS f2*)(L + L_B + lane * 8);
      const float r = rbf((fin[0] + fin[8]) + (fin[16] + fin[24]));
      for (int i = 0; i < 400000 && (int)(clock32() - hold0) < 6000; ++i) {}
      const unsigned short ov = (unsigned short)pk(rbf(bx.y + r) + bx.x, 0.f);
      if (OSTORE == 0) *po = ov;
      else if (OSTORE == 1) __hip_atomic_store(po, ov, __ATOMIC_RELAXED, __HIP_MEMORY_SCOPE_AGENT);
      else __builtin_nontemporal_store(ov, po);
      if (b == 0 && lane == 0) __builtin_amdgcn_raw_buffer_store_b64(e, rs, WS_EPOCH, 0, SC1);
    }
    STAMP(11);
    if (wave == 0) FLUSH(10, 11);
    return;
  }

  const int rho = lane >> 3, c = lane & 7;
  if (wave == 4) {
    const int kq = s & 3, strip = s >> 2;
    const int r0 = kq * 128 + 16 * rho;
    const v4u xa = *(const GAS v4u*)(p.x + 8 * lane);
    const v4u xb0 = *(const GAS v4u*)(p.x + r0), xb1 = *(const GAS v4u*)(p.x + r0 + 8);
    const v2u eo = __builtin_amdgcn_raw_buffer_load_b64(rs, WS_EPOCH, 0, SC1);
    const unsigned short* wp = p.wq + (size_t)r0 * NQ + gx * 512 + strip * 64 + c * 8;
    v4u w[16];
#pragma unroll
    for (int i = 0; i < 16; ++i) w[i] = __builtin_nontemporal_load((const GAS v4u*)(wp + (size_t)i * NQ));
    __builtin_amdgcn_sched_barrier(0);
    __builtin_amdgcn_s_barrier();
    STAMP(12);
    float mean, rden;
    ln_stats(xa, mean, rden);
    const v4u n0 = ln_apply(xb0, mean, rden), n1 = ln_apply(xb1, mean, rden);
    v2u e = {uni(eo.x) + 1u, uni(eo.y) + 1u};
    if (e.x == 0u) e.x = 1u;
    if (e.y == 0u) e.y = 1u;
    float acc[8] = {0.f, 0.f, 0.f, 0.f, 0.f, 0.f, 0.f, 0.f};
#pragma unroll
    for (int i = 0; i < 4; ++i) mac(pair_rows(w[2 * i], w[2 * i + 1]), n0[i], acc);
#pragma unroll
    for (int i = 0; i < 4; ++i) mac(pair_rows(w[8 + 2 * i], w[9 + 2 * i]), n1[i], acc);
    const f2 u = reduce8(acc);
    if ((rho & 1) == 0) put2(rs, WS_Q + (gx * 4 + kq) * 4096 + (strip * 32 + (lane >> 4) * 8 + c) * 16, asu(u.x), asu(u.y), e);
    STAMP(4);
    int off[16];
#pragma unroll
    for (int i = 0; i < 16; ++i) off[i] = WS_Q + (g * 4 + (i & 3)) * 4096 + ((i >> 2) * 64 + lane) * 16;
    v4u q[16];
    gather<16, false>(rs, off, e, q);
#pragma unroll
    for (int i = 0; i < 4; ++i)
      *(LAS unsigned*)(L + L_Q + (i * 64 + (lane & 32) + 4 * c + (rho & 3)) * 4) =
          pk((asf(q[4 * i].x) + asf(q[4 * i + 1].x)) + (asf(q[4 * i + 2].x) + asf(q[4 * i + 3].x)),
             (asf(q[4 * i].y) + asf(q[4 * i + 1].y)) + (asf(q[4 * i + 2].y) + asf(q[4 * i + 3].y)));
    if (lane == 0) *(LAS v2u*)(L + L_E) = e;
    asm volatile("s_waitcnt lgkmcnt(0)" ::: "memory");
    if (lane == 0) *(volatile LAS unsigned*)(L + L_FLAG) = 1u;
    STAMP(6);
    FLUSH(4, 4); FLUSH(6, 6); FLUSH(12, 12);
    if (ADAPT) {
      const bool rd = lane < 8 && lane != g;
      unsigned last = 0, t_ref = 0, ref = 0, t_first = 0, ref_first = 0;
      float drift = 0.f;
      int nref = 0;
#pragma unroll 1
      for (int it = 0; it < 8192; ++it) {
        const bool exch = it % NEXCH == 0 && last < XSTOP;
        v4u t = {0u, 0u, 0u, 0u};
        asm volatile("" ::: "memory");
        if (exch && it && rd) t = __builtin_amdgcn_raw_buffer_load_b128(rs, WS_PL + (((s + 5 * (it / NEXCH) + 3 * lane) & (31 & ~PUBM)) * 8 + lane) * 128, 0, SC1);
        asm volatile("" : "+v"(t) :: "memory");
        last = progress(L);
        const unsigned now = clock32(), lag = now - (last * TAU16 >> 4);
        if (exch) {
          if (lane == 0 && (s & PUBM) == 0) put2(rs, WS_PL + b * 128, lag, now, e);
          const bool ok = rd && (t.x ^ t.z) == e.x && (t.y ^ t.w) == e.y;
          float sl = ok ? (float)(int)(t.x - lag) : 0.f, st = ok ? (float)(int)(t.y - now) : 0.f;
          sl += dpp<X1>(sl); st += dpp<X1>(st); sl += dpp<X2>(sl); st += dpp<X2>(st); sl += dpp<X7>(sl); st += dpp<X7>(st);
          const int n = __builtin_popcountll(__builtin_amdgcn_ballot_w64(ok));
          if (n >= 4) {
            const float w = rcp((float)(n + 1));
            ref = lag + uni((unsigned)(int)(sl * w));
            t_ref = now + uni((unsigned)(int)(st * w));
            if (nref == 1) { ref_first = ref; t_first = t_ref; }
            else if (nref > 1) drift = (float)(int)(ref - ref_first) * rcp((float)(int)(t_ref - t_first) + 200.f);
            ++nref;
          }
        }
        const int lead = nref ? (int)(ref - lag + uni((unsigned)(int)(drift * (float)(int)(now - t_ref)))) : 0;
        if (last >= 4 * (NSTEP - RING)) {
          if (lane == 0) *(volatile LAS unsigned*)(L + L_TEND) = lag + (unsigned)lead + (4 * (NSTEP - RING) * TAU16 >> 4);
          break;
        }
        if (lane == 0) *(volatile LAS unsigned*)(L + L_LVL) = (unsigned)min(max((UNOM - lead * KP) >> 8, 0), 7);
        __builtin_amdgcn_s_sleep(CSLEEP);
      }
      asm volatile("s_waitcnt lgkmcnt(0)" ::: "memory");
    }
    return;
  }

  if (wave == 5) {
    const v4u cpf = __builtin_amdgcn_raw_buffer_load_b128(
        __builtin_amdgcn_make_buffer_rsrc((void*)code, 0, code_len > 0 && code_len <= 32768 ? code_len & ~15 : 0, 0x00020000), s * 1024 + lane * 16, 0, 0);
    __builtin_amdgcn_sched_barrier(0);
    __builtin_amdgcn_s_barrier();
    asm volatile("" :: "v"(cpf));
    wait_word<4>(L + L_CNT);
    Pairs w[8];
#pragma unroll
    for (int j = 0; j < 8; ++j) {
      w[j] = pair_rows(*(const LAS v4u*)(L + L_WO + 2 * j * 1024 + lane * 16), *(const LAS v4u*)(L + L_WO + (2 * j + 1) * 1024 + lane * 16));
#pragma unroll
      for (int k = 0; k < 8; ++k) pin(w[j].c[k]);
    }
    int off[4], a_pa = WS_A + (g * 32 + s) * ROW + lane * 16;
#pragma unroll
    for (int i = 0; i < 4; ++i) { off[i] = WS_O + (gx * 32 + rho + 8 * i) * ROW + s * 128 + c * 16; pin(off[i]); }
    pin(a_pa);
    lds_barrier();
    const v2u e = *(const LAS v2u*)(L + L_E);
    const unsigned t_end = uni(*(volatile LAS unsigned*)(L + L_TEND));
    STAMP(16);
    if (D1) __builtin_amdgcn_s_sleep(D1);
    if (T1) wait_until(t_end + T1);
    v4u t[4];
    gather<4, true>(rs, off, e, t);
    STAMP(8);
    const float m0 = dpp<0xA0>(asf(t[0].z)), m1 = dpp<0xA0>(asf(t[1].z)), m2 = dpp<0xA0>(asf(t[2].z)), m3 = dpp<0xA0>(asf(t[3].z));
    float M = fmaxf(fmaxf(m0, m1), fmaxf(m2, m3));
    M = rows_max(fmaxf(M, dpp<R8>(M)));
    const float f0 = ex2((m0 - M) * LOG2E), f1 = ex2((m1 - M) * LOG2E), f2_ = ex2((m2 - M) * LOG2E), f3 = ex2((m3 - M) * LOG2E);
    float o0 = fmaf(f0, asf(t[0].x), f1 * asf(t[1].x)) + fmaf(f2_, asf(t[2].x), f3 * asf(t[3].x));
    float o1 = fmaf(f0, asf(t[0].y), f1 * asf(t[1].y)) + fmaf(f2_, asf(t[2].y), f3 * asf(t[3].y));
    float ls = dpp<0xF5>(fmaf(f0, asf(t[0].z), f1 * asf(t[1].z)) + fmaf(f2_, asf(t[2].z), f3 * asf(t[3].z)));
    o0 += dpp<R8>(o0); o1 += dpp<R8>(o1); ls += dpp<R8>(ls);
    o0 = rows_add(o0); o1 = rows_add(o1); ls = rcp(rows_add(ls));
    const unsigned op = pk(o0 * ls, o1 * ls);
    unsigned oj[8];
#pragma unroll
    for (int j = 0; j < 8; ++j) oj[j] = __builtin_amdgcn_readlane(op, j);
    __builtin_amdgcn_sched_barrier(0);
    float acc[8] = {0.f, 0.f, 0.f, 0.f, 0.f, 0.f, 0.f, 0.f};
#pragma unroll
    for (int j = 0; j < 8; ++j) mac(w[j], oj[j], acc);
#pragma unroll
    for (int k = 0; k < 4; ++k) put2(rs, a_pa + k * 1024, asu(acc[2 * k]), asu(acc[2 * k + 1]), e);
    if (lane == 0) *(volatile LAS unsigned*)(L + L_FLAG) = 2u;
    STAMP(9);
    FLUSH(8, 9); FLUSH(16, 17);
    return;
  }

  __builtin_amdgcn_s_barrier();
  if (s >= NFFN) return;
  const int id = (s & 7) * 8 + g;
  const v4u xa = *(const GAS v4u*)(p.x + 8 * lane);
  const v4u wr = *(const GAS v4u*)(p.wr + 8 * lane);
  const unsigned xx = *(const GAS unsigned*)(p.x + 2 * (16 * (id & 15) + 4 * (lane & 3) + (id >> 4)));
  const v4u xb = *(const GAS v4u*)(p.x + g * 64 + 8 * rho);
  float mean, rden;
  ln_stats(xa, mean, rden);
  wait_word<4>(L + L_CNT);
  Pairs wd[4];
  unsigned gu0, gu1;
  {
    const v4u n = ln_apply(xb, mean, rden);
    float ga[8] = {0.f, 0.f, 0.f, 0.f, 0.f, 0.f, 0.f, 0.f}, ua[8] = {0.f, 0.f, 0.f, 0.f, 0.f, 0.f, 0.f, 0.f};
#pragma unroll
    for (int i = 0; i < 4; ++i) {
      const LAS unsigned char* w = L + 2 * i * 1024 + lane * 16;
      mac(pair_rows(*(const LAS v4u*)(w + L_WG), *(const LAS v4u*)(w + L_WG + 1024)), n[i], ga);
      mac(pair_rows(*(const LAS v4u*)(w + L_WU), *(const LAS v4u*)(w + L_WU + 1024)), n[i], ua);
      wd[i] = pair_rows(*(const LAS v4u*)(w + L_WD), *(const LAS v4u*)(w + L_WD + 1024));
#pragma unroll
      for (int k = 0; k < 8; ++k) pin(wd[i].c[k]);
    }
    const f2 gs = reduce8(ga), us = reduce8(ua);
    gu0 = asu((rho & 1) ? us.x : gs.x); gu1 = asu((rho & 1) ? us.y : gs.y);
  }
  const v4u nx = ln_apply(xa, mean, rden);
  const float r = rbf(wave_sum(dot2(nx.x, wr.x, dot2(nx.y, wr.y, 0.f)) + dot2(nx.z, wr.z, dot2(nx.w, wr.w, 0.f))));
  float sg = rbf(rcp(1.0f + ex2(-LOG2E * r)));
  const unsigned xnp = ln_pair(xx, mean, rden);
  float xn1 = lane < 32 ? lo(xnp) : hi(xnp), x1 = lane < 32 ? lo(xx) : hi(xx);
  int og[8], oy[2], a_py = WS_Y + s * ROW + (g * 4 + (lane >> 4)) * 128 + c * 16;
#pragma unroll
  for (int i = 0; i < 8; ++i) og[i] = WS_GU + (s * 8 + i) * 1024 + lane * 16;
  const bool two = (lane >> 2) + 16 < NFFN;
  oy[0] = WS_Y + (((id & 15) >> 1) * 4 + (id >> 4)) * 128 + (4 * (id & 1) + (lane & 3)) * 16 + (lane >> 2) * ROW;
  oy[1] = oy[0] + (two ? 16 * ROW : 0);
  pin(gu0); pin(gu1); pin(sg); pin(xn1); pin(x1); pin(oy[0]); pin(oy[1]); pin(a_py);
  wait_word<1>(L + L_FLAG);
  const v2u e = *(const LAS v2u*)(L + L_E);
#pragma unroll 1
  for (int it = 0; it < 65536 && progress(L) < GPROG; ++it) __builtin_amdgcn_s_sleep(4);
  put2(rs, WS_GU + (s * 8 + g) * 1024 + lane * 16, gu0, gu1, e);
  lds_barrier();
  v4u gu[8];
  gather<8, false>(rs, og, e, gu);
  const unsigned ap = pk(((asf(gu[0].x) + asf(gu[1].x)) + (asf(gu[2].x) + asf(gu[3].x))) + ((asf(gu[4].x) + asf(gu[5].x)) + (asf(gu[6].x) + asf(gu[7].x))),
                         ((asf(gu[0].y) + asf(gu[1].y)) + (asf(gu[2].y) + asf(gu[3].y))) + ((asf(gu[4].y) + asf(gu[5].y)) + (asf(gu[6].y) + asf(gu[7].y))));
  const float a0 = lo(ap), a1 = hi(ap);
  const unsigned sp = pk(a0 * rcp(1.0f + ex2(-LOG2E * a0)), a1 * rcp(1.0f + ex2(-LOG2E * a1)));
  const int hp = (int)pk(lo(sp) * dpp<R8>(a0), hi(sp) * dpp<R8>(a1));
  float y[8] = {0.f, 0.f, 0.f, 0.f, 0.f, 0.f, 0.f, 0.f};
#pragma unroll
  for (int i = 0; i < 4; ++i) mac(wd[i], (unsigned)__builtin_amdgcn_ds_bpermute((16 * i + rho) * 4, hp), y);
  const f2 ys = reduce8(y);
  if ((rho & 1) == 0) put2(rs, a_py, asu(ys.x), asu(ys.y), e);
  STAMP(5);
  FLUSH(5, 5);
  if (s >= 8) return;
  __builtin_amdgcn_s_sleep(DY);
  v4u yv[2];
  gather<2, false>(rs, oy, e, yv);
  const float yc = rbf(reduce_quads(asf(yv[0].x) + (two ? asf(yv[1].x) : 0.f), asf(yv[0].y) + (two ? asf(yv[1].y) : 0.f)));
  if ((lane & 28) == 0) *(LAS f2*)(L + L_B + ((lane >> 5) * 4 + (lane & 3)) * 8) = f2{rbf(xn1 + rbf(yc * sg)), x1};
  asm volatile("s_waitcnt lgkmcnt(0)" ::: "memory");
  STAMP(7);
  FLUSH(7, 7);
}

extern "C" __device__ __attribute__((used, noinline)) void END_MARK() {}

extern "C" void kernel_launch(void* const* d_in, const int* in_sizes, int n_in, void* d_out, int out_size, void* d_ws, size_t ws_size,
                              hipStream_t stream) {
  (void)in_sizes; (void)n_in; (void)out_size;
  Params p;
  p.x = (const unsigned short*)d_in[0];
  p.k = (const unsigned short*)d_in[1];
  p.v = (const unsigned short*)d_in[2];
  p.wq = (const unsigned short*)d_in[3];
  p.wo = (const unsigned short*)d_in[4];
  p.wr = (const unsigned short*)d_in[6];
  p.wg = (const unsigned short*)d_in[7];
  p.wu = (const unsigned short*)d_in[8];
  p.wd = (const unsigned short*)d_in[9];
  p.out = (unsigned*)d_out;
  p.ws = (unsigned char*)d_ws + (WSTAIL ? (ws_size - WS_SPAN) / 4096 * 4096 : 0);
  hipLaunchKernelGGL(KERNEL_NAME, dim3(NWG), dim3(NTHR), 0, stream, p);
}
```

```cpp
#include <hip/hip_runtime.h>
#include <hip/hip_bf16.h>
#include <stdint.h>

#define GAS __attribute__((address_space(1)))
#define LAS __attribute__((address_space(3)))
typedef unsigned v4u __attribute__((ext_vector_type(4)));
typedef unsigned v2u __attribute__((ext_vector_type(2)));
typedef float f4 __attribute__((ext_vector_type(4)));
typedef float f2 __attribute__((ext_vector_type(2)));
typedef short s16x4 __attribute__((ext_vector_type(4)));
typedef __bf16 bf16x8 __attribute__((ext_vector_type(8)));
typedef __bf16 bf16x2 __attribute__((ext_vector_type(2)));

#define END_MARK fused_decode_code_end
#define KERNEL_NAME fused_decode_kernel
#define STR2(x) #x
#define STR(x) STR2(x)
#define NSTEP_RUN NSTEP
#define HOLDT 0
#define WSTAIL 1
#define ADAPT 1
#define PACE0 4
#define KP 6
#define UNOM 576
#define NEXCH 8
#define XSTOP 208
#define PUBM 3
#define CSLEEP 10
#define TAU16 218
#define D1 18
#define D2 10
#define DY 10
#define T1 0
#define T2 0
#define GPROG 240

namespace {

constexpr int D = 512, DH = 128, SK = 65536, NQ = 4096, FF = 1792;
constexpr int NWG = 256, NTHR = 448;
constexpr int KEYS_PER_WG = SK / 32;
constexpr int NSTEP = KEYS_PER_WG / 32;
constexpr int RING = 4;
constexpr int NFFN = FF / 64;
constexpr float SCALE = 0.08838834764831845f, LOG2E = 1.4426950408889634f, FMIN = -3.4028234664e38f;
constexpr unsigned SPIN_CAP = 1u << 16;

constexpr int ROW = 4096 + 128;
constexpr int WS_EPOCH = 0;
constexpr int WS_Q = 4096;
constexpr int WS_GU = WS_Q + 8 * 4 * 4096;
constexpr int WS_Y = WS_GU + NFFN * 8192;
constexpr int WS_O = WS_Y + NFFN * ROW;
constexpr int WS_A = WS_O + 256 * ROW;
constexpr int WS_PL = WS_A + 256 * ROW;
constexpr int WS_END = WS_PL + 256 * 128;
constexpr size_t WS_SPAN = 4u << 20;
static_assert(WS_END + 65536 <= WS_SPAN, "workspace map");

constexpr int L_Q = 0, L_E = 1024, L_FLAG = 1032, L_CNT = 1036, L_PROG = 1040, L_LVL = 1056, L_TEND = 1060, L_B = 1072, L_FIN = 1136;
constexpr int L_ML = 1280;
constexpr int REC = 4 * 132 * 4, L_REC = 1536;
constexpr int L_WO = 16384, L_WG = 32768, L_WU = 40960, L_WD = 49152;
constexpr int LDS_BYTES = 84 * 1024;

struct Params {
  const unsigned short *x, *k, *v, *wq, *wo, *wr, *wg, *wu, *wd;
  unsigned* out;
  unsigned char* ws;
};

__device__ __forceinline__ float asf(unsigned u) { return __builtin_bit_cast(float, u); }
__device__ __forceinline__ unsigned asu(float f) { return __builtin_bit_cast(unsigned, f); }
__device__ __forceinline__ float lo(unsigned u) { return asf(u << 16); }
__device__ __forceinline__ float hi(unsigned u) { return asf(u & 0xffff0000u); }
__device__ __forceinline__ unsigned pk(float a, float b) {
  bf16x2 r = {(__bf16)a, (__bf16)b};
  return __builtin_bit_cast(unsigned, r);
}
__device__ __forceinline__ float rbf(float a) { return lo(pk(a, a)); }
__device__ __forceinline__ float rcp(float a) { return __builtin_amdgcn_rcpf(a); }
__device__ __forceinline__ float ex2(float a) { return __builtin_amdgcn_exp2f(a); }
__device__ __forceinline__ unsigned xor3(unsigned a, unsigned b, unsigned c) { return __builtin_amdgcn_bitop3_b32(a, b, c, 0x96); }
__device__ __forceinline__ unsigned or3(unsigned a, unsigned b, unsigned c) { return __builtin_amdgcn_bitop3_b32(a, b, c, 0xFE); }
__device__ __forceinline__ unsigned uni(unsigned a) { return (unsigned)__builtin_amdgcn_readfirstlane((int)a); }
__device__ __forceinline__ unsigned clock32() { return (unsigned)__builtin_amdgcn_s_memrealtime(); }
__device__ __forceinline__ float dot2(unsigned a, unsigned b, float c) {
  return __builtin_amdgcn_fdot2_f32_bf16(__builtin_bit_cast(bf16x2, a), __builtin_bit_cast(bf16x2, b), c, false);
}
template <class T> __device__ __forceinline__ void pin(T& x) { asm volatile("" : "+v"(x)); }
template <int CTRL> __device__ __forceinline__ float dpp(float x) {
  return asf(__builtin_amdgcn_mov_dpp(__builtin_bit_cast(int, x), CTRL, 0xf, 0xf, true));
}
constexpr int X1 = 0xB1, X2 = 0x4E, X7 = 0x141, R4 = 0x124, R8 = 0x128;
__device__ __forceinline__ float x32_tr(float a, float b) {
  auto s = __builtin_amdgcn_permlane32_swap(asu(a), asu(b), false, false);
  return asf(s[0]) + asf(s[1]);
}
__device__ __forceinline__ float x16_tr(float a, float b) {
  auto s = __builtin_amdgcn_permlane16_swap(asu(a), asu(b), false, false);
  return asf(s[0]) + asf(s[1]);
}
__device__ __forceinline__ float rows_add(float x) { x = x16_tr(x, x); return x32_tr(x, x); }
__device__ __forceinline__ float rows_max(float x) {
  auto s = __builtin_amdgcn_permlane16_swap(asu(x), asu(x), false, false);
  x = fmaxf(asf(s[0]), asf(s[1]));
  auto t = __builtin_amdgcn_permlane32_swap(asu(x), asu(x), false, false);
  return fmaxf(asf(t[0]), asf(t[1]));
}
__device__ __forceinline__ float wave_sum(float x) {
  x += dpp<X1>(x); x += dpp<X2>(x); x += dpp<X7>(x); x += dpp<R8>(x);
  return rows_add(x);
}
__device__ __forceinline__ void lds_barrier() {
  asm volatile("s_waitcnt lgkmcnt(0)" ::: "memory");
  __builtin_amdgcn_s_barrier();
  asm volatile("" ::: "memory");
}
template <int N> __device__ __forceinline__ void pace() {
  __builtin_amdgcn_sched_barrier(0);
  asm volatile("s_waitcnt vmcnt(%0)" :: "i"(N) : "memory");
  __builtin_amdgcn_sched_barrier(0);
}
__device__ __forceinline__ void pace_lvl(unsigned lvl) {
  __builtin_amdgcn_sched_barrier(0);
  asm volatile("s_cmp_gt_u32 %0, 3\n\ts_cbranch_scc1 4f\n\ts_cmp_gt_u32 %0, 1\n\ts_cbranch_scc1 2f\n\t"
               "s_cmp_eq_u32 %0, 1\n\ts_cbranch_scc1 1f\n\ts_waitcnt vmcnt(2)\n1:\n\ts_waitcnt vmcnt(3)\n\ts_branch 9f\n"
               "2:\n\ts_cmp_eq_u32 %0, 3\n\ts_cbranch_scc1 3f\n\ts_waitcnt vmcnt(4)\n3:\n\ts_waitcnt vmcnt(5)\n\ts_branch 9f\n"
               "4:\n\ts_cmp_gt_u32 %0, 5\n\ts_cbranch_scc1 6f\n\t"
               "s_cmp_eq_u32 %0, 5\n\ts_cbranch_scc1 5f\n\ts_waitcnt vmcnt(6)\n5:\n\ts_waitcnt vmcnt(7)\n\ts_branch 9f\n"
               "6:\n\ts_cmp_gt_u32 %0, 7\n\ts_cbranch_scc1 9f\n\t"
               "s_cmp_eq_u32 %0, 7\n\ts_cbranch_scc1 7f\n\ts_waitcnt vmcnt(8)\n7:\n\ts_waitcnt vmcnt(10)\n9:"
               :: "s"(lvl) : "scc");
  __builtin_amdgcn_sched_barrier(0);
}
constexpr int SC1 = 16, NT = 2;
__device__ __forceinline__ void put2(__amdgpu_buffer_rsrc_t rs, int off, unsigned a, unsigned b, v2u e) {
  __builtin_amdgcn_raw_buffer_store_b128(v4u{a, b, a ^ e.x, b ^ e.y}, rs, off, 0, SC1);
}
__device__ __forceinline__ void put3(__amdgpu_buffer_rsrc_t rs, int off, unsigned a, unsigned b, unsigned c, v2u e) {
  __builtin_amdgcn_raw_buffer_store_b128(v4u{a, b, c, xor3(a, b, c) ^ e.x}, rs, off, 0, SC1);
}
template <int N, bool THREE> __device__ __forceinline__ void gather(__amdgpu_buffer_rsrc_t rs, const int (&off)[N], v2u e, v4u (&g)[N]) {
#pragma unroll 1
  for (unsigned spin = 0; spin < SPIN_CAP; ++spin) {
    asm volatile("" ::: "memory");
#pragma unroll
    for (int i = 0; i < N; ++i) g[i] = __builtin_amdgcn_raw_buffer_load_b128(rs, off[i], 0, SC1);
    unsigned bad = 0;
#pragma unroll
    for (int i = 0; i < N; ++i) {
      if (THREE) bad |= xor3(xor3(g[i].x, g[i].y, g[i].z), g[i].w, e.x);
      else bad = or3(xor3(g[i].x, g[i].z, e.x), xor3(g[i].y, g[i].w, e.y), bad);
    }
    if (__builtin_amdgcn_ballot_w64(bad != 0u) == 0ull) break;
  }
}
template <unsigned V> __device__ __forceinline__ void wait_word(LAS unsigned char* w) {
#pragma unroll 1
  for (unsigned spin = 0; spin < (1u << 22); ++spin) {
    if (uni(*(volatile LAS unsigned*)w) >= V) break;
    __builtin_amdgcn_s_sleep(1);
  }
  asm volatile("" ::: "memory");
}
__device__ __forceinline__ unsigned progress(LAS unsigned char* L) {
  const v4u pr = *(volatile LAS v4u*)(L + L_PROG);
  return uni(pr.x + pr.y + pr.z + pr.w);
}
__device__ __forceinline__ void wait_until(unsigned t) {
#pragma unroll 1
  for (int i = 0; i < 128 && (int)(clock32() - t) < 0; ++i) {}
}
__device__ __forceinline__ void dma(const void* src, unsigned dst) {
  unsigned keep;
  asm volatile("s_mov_b32 %0, m0\n\ts_mov_b32 m0, %2\n\ts_nop 0\n\tglobal_load_lds_dwordx4 %1, off nt\n\ts_mov_b32 m0, %0"
               : "=&s"(keep) : "v"(src), "s"(dst) : "memory");
}
__device__ __forceinline__ void ln_stats(v4u xa, float& mean, float& rden) {
  const unsigned one = 0x3f803f80u;
  const float mu = wave_sum(dot2(xa.x, one, dot2(xa.y, one, 0.f)) + dot2(xa.z, one, dot2(xa.w, one, 0.f))) * (1.0f / D);
  float q0 = 0.f, q1 = 0.f;
#pragma unroll
  for (int j = 0; j < 4; ++j) { const float a = lo(xa[j]) - mu, b = hi(xa[j]) - mu; q0 = fmaf(a, a, q0); q1 = fmaf(b, b, q1); }
  const float var = rbf(wave_sum(q0 + q1) * (1.0f / D));
  mean = rbf(mu);
  rden = rcp(rbf(__builtin_amdgcn_sqrtf(rbf(var + 1e-5f))));
}
__device__ __forceinline__ unsigned ln_pair(unsigned xx, float mean, float rden) {
  const unsigned d = pk(lo(xx) - mean, hi(xx) - mean);
  return pk(lo(d) * rden, hi(d) * rden);
}
__device__ __forceinline__ v4u ln_apply(v4u xa, float mean, float rden) {
  return v4u{ln_pair(xa.x, mean, rden), ln_pair(xa.y, mean, rden), ln_pair(xa.z, mean, rden), ln_pair(xa.w, mean, rden)};
}
struct Pairs { unsigned c[8]; };
__device__ __forceinline__ Pairs pair_rows(v4u w0, v4u w1) {
  Pairs r;
#pragma unroll
  for (int k = 0; k < 4; ++k) {
    r.c[2 * k] = __builtin_amdgcn_perm(w1[k], w0[k], 0x05040100u);
    r.c[2 * k + 1] = __builtin_amdgcn_perm(w1[k], w0[k], 0x07060302u);
  }
  return r;
}
__device__ __forceinline__ void mac(const Pairs& w, unsigned x, float (&acc)[8]) {
#pragma unroll
  for (int k = 0; k < 8; ++k) acc[k] = dot2(w.c[k], x, acc[k]);
}
__device__ __forceinline__ f2 reduce8(const float (&acc)[8]) {
  const float t0 = x32_tr(acc[0], acc[4]), t1 = x32_tr(acc[1], acc[5]), t2 = x32_tr(acc[2], acc[6]), t3 = x32_tr(acc[3], acc[7]);
  const float u0 = x16_tr(t0, t2), u1 = x16_tr(t1, t3);
  return f2{u0 + dpp<R8>(u0), u1 + dpp<R8>(u1)};
}
__device__ __forceinline__ float reduce_quads(float a, float b) {
  float t = x32_tr(a, b);
  t = x16_tr(t, t);
  t += dpp<R4>(t);
  return t + dpp<R8>(t);
}
#define STAMP(i) do {} while (0)
#define FLUSH(a, b) do {} while (0)

struct Acc { f4 o0, o1; float m, l; };

__device__ __forceinline__ unsigned scores_to_p(v4u ka, v4u kb, bf16x8 qf, Acc& s) {
  const f4 z = {0.f, 0.f, 0.f, 0.f};
  const f4 da = __builtin_amdgcn_mfma_f32_16x16x32_bf16(__builtin_bit_cast(bf16x8, ka), qf, z, 0, 0, 0);
  const f4 db = __builtin_amdgcn_mfma_f32_16x16x32_bf16(__builtin_bit_cast(bf16x8, kb), qf, z, 0, 0, 0);
  float sa = (dpp<0x00>(da[0]) + dpp<0x55>(da[1])) + (dpp<0xAA>(da[2]) + dpp<0xFF>(da[3]));
  float sb = (dpp<0x00>(db[0]) + dpp<0x55>(db[1])) + (dpp<0xAA>(db[2]) + dpp<0xFF>(db[3]));
  unsigned u = pk(sa, sb);
  u = pk(lo(u) * SCALE, hi(u) * SCALE);
  sa = lo(u); sb = hi(u);
  float mx = fmaxf(sa, sb);
  if (__builtin_amdgcn_ballot_w64(mx > s.m) != 0ull) {
    mx = fmaxf(s.m, rows_max(mx));
    const float al = ex2((s.m - mx) * LOG2E);
    s.o0 *= al; s.o1 *= al; s.l *= al; s.m = mx;
  }
  const float pa = ex2((sa - s.m) * LOG2E), pb = ex2((sb - s.m) * LOG2E);
  s.l += pa + pb;
  return pk(pa, pb);
}
__device__ __forceinline__ void pv(v4u va, v4u vb, unsigned pp, v4u sel, Acc& s) {
  const v2u ba = {__builtin_amdgcn_perm(pp, pp, sel.x), __builtin_amdgcn_perm(pp, pp, sel.y)};
  const v2u bb = {__builtin_amdgcn_perm(pp, pp, sel.z), __builtin_amdgcn_perm(pp, pp, sel.w)};
  s.o0 = __builtin_amdgcn_mfma_f32_16x16x16bf16_1k(__builtin_bit_cast(s16x4, v2u{va.x, va.y}), __builtin_bit_cast(s16x4, ba), s.o0, 0, 0, 0);
  s.o1 = __builtin_amdgcn_mfma_f32_16x16x16bf16_1k(__builtin_bit_cast(s16x4, v2u{va.z, va.w}), __builtin_bit_cast(s16x4, ba), s.o1, 0, 0, 0);
  s.o0 = __builtin_amdgcn_mfma_f32_16x16x16bf16_1k(__builtin_bit_cast(s16x4, v2u{vb.x, vb.y}), __builtin_bit_cast(s16x4, bb), s.o0, 0, 0, 0);
  s.o1 = __builtin_amdgcn_mfma_f32_16x16x16bf16_1k(__builtin_bit_cast(s16x4, v2u{vb.z, vb.w}), __builtin_bit_cast(s16x4, bb), s.o1, 0, 0, 0);
}

}

extern "C" __global__ void __launch_bounds__(NTHR, 2) KERNEL_NAME(Params p) {
  __shared__ __attribute__((aligned(16))) unsigned char smem[LDS_BYTES];
  LAS unsigned char* L = (LAS unsigned char*)smem;
  unsigned long long code;
  int code_len;
  asm volatile("s_getpc_b64 %0\n\ts_mov_b32 %1, " STR(END_MARK) "@rel32@lo+4" : "=s"(code), "=s"(code_len));
  const int lane = threadIdx.x & 63, wave = __builtin_amdgcn_readfirstlane(threadIdx.x >> 6);
  const int b = blockIdx.x, g = b & 7, s = b >> 3;
  const int gx = g ^ 1;
  const __amdgpu_buffer_rsrc_t rs = __builtin_amdgcn_make_buffer_rsrc((void*)p.ws, 0, WS_END, 0x00020000);

  if (wave < 4) {
    STAMP(0);
    const size_t base = ((size_t)g * SK + (size_t)s * KEYS_PER_WG) * DH;
    const __amdgpu_buffer_rsrc_t rk = __builtin_amdgcn_make_buffer_rsrc((void*)(p.k + base), 0, KEYS_PER_WG * DH * 2, 0x00020000);
    const __amdgpu_buffer_rsrc_t rv = __builtin_amdgcn_make_buffer_rsrc((void*)(p.v + base), 0, KEYS_PER_WG * DH * 2, 0x00020000);
    const int h = (lane >> 2) & 3, c = lane & 3;
    const int frag = h * 256 + (lane >> 4) * 64 + c * 16;
    int ko = wave * 2048 + frag, vo = wave * 2048 + lane * 16;
    constexpr int STRIDE = 32 * DH * 2;
    if (threadIdx.x == 0) {
      *(volatile LAS v2u*)(L + L_FLAG) = v2u{0u, 0u};
      *(volatile LAS v4u*)(L + L_PROG) = v4u{0u, 0u, 0u, 0u};
      *(volatile LAS v2u*)(L + L_LVL) = v2u{2u, 0u};
    }
    lds_barrier();
    {
      const unsigned lb = (unsigned)(size_t)L;
      const unsigned short* wo = p.wo + (size_t)(gx * 512 + s * 16 + 4 * wave) * D + 8 * lane;
#pragma unroll
      for (int i = 0; i < 4; ++i) dma(wo + i * D, lb + L_WO + (4 * wave + i) * 1024);
      if (s < NFFN) {
        const int row = 8 * (lane >> 3) + 2 * wave;
        const size_t gu = (size_t)(g * 64 + row) * FF + s * 64 + (lane & 7) * 8;
        const unsigned short* wd = p.wd + (size_t)(s * 64 + row) * D + g * 64 + (lane & 7) * 8;
        dma(p.wg + gu, lb + L_WG + 2 * wave * 1024);
        dma(p.wu + gu, lb + L_WU + 2 * wave * 1024);
        pace<PACE0>();
        dma(p.wg + gu + FF, lb + L_WG + (2 * wave + 1) * 1024);
        dma(p.wu + gu + FF, lb + L_WU + (2 * wave + 1) * 1024);
        pace<PACE0>();
        dma(wd, lb + L_WD + 2 * wave * 1024);
        dma(wd + D, lb + L_WD + (2 * wave + 1) * 1024);
      }
    }
    v4u ka[RING], kb[RING], va[RING], vb[RING];
#pragma unroll
    for (int r = 0; r < RING; ++r) {
      pace<PACE0>();
      if (r == (PACE0 + 3) / 4 && lane == 0)
        __hip_atomic_fetch_add((LAS unsigned*)(L + L_CNT), 1u, __ATOMIC_RELAXED, __HIP_MEMORY_SCOPE_WORKGROUP);
      ka[r] = __builtin_amdgcn_raw_buffer_load_b128(rk, ko, 0, NT);
      kb[r] = __builtin_amdgcn_raw_buffer_load_b128(rk, ko + 1024, 0, NT);
      pace<PACE0>();
      va[r] = __builtin_amdgcn_raw_buffer_load_b128(rv, vo, 0, NT);
      vb[r] = __builtin_amdgcn_raw_buffer_load_b128(rv, vo + 1024, 0, NT);
      ko += STRIDE; vo += STRIDE;
    }
    __builtin_amdgcn_sched_barrier(0);
    const unsigned z = 0x0c0c0c0cu;
    const v4u sel = {c == 0 ? 0x0c0c0100u : c == 1 ? 0x01000c0cu : z, c == 2 ? 0x0c0c0100u : c == 3 ? 0x01000c0cu : z,
                     c == 0 ? 0x0c0c0302u : c == 1 ? 0x03020c0cu : z, c == 2 ? 0x0c0c0302u : c == 3 ? 0x03020c0cu : z};
    const int id = s * 8 + g;
    int a_ml = L_ML + h * 32 + wave * 8, a_ow = L_REC + wave * REC + (h * 132 + (lane >> 4) * 32 + c) * 4, a_or = L_REC + (wave * 132 + 2 * lane) * 4;
    int a_po = WS_O + (g * 32 + s) * ROW + threadIdx.x * 16;
    pin(a_ml); pin(a_ow); pin(a_or); pin(a_po);
    STAMP(14);
    wait_word<1>(L + L_FLAG);
    const bf16x8 qf = __builtin_bit_cast(bf16x8, *(const LAS v4u*)(L + L_Q + frag));
    v2u e = *(const LAS v2u*)(L + L_E);
    pin(e);
    STAMP(1);
    Acc a;
    a.o0 = f4{0.f, 0.f, 0.f, 0.f}; a.o1 = a.o0; a.m = FMIN; a.l = 0.f;
#pragma unroll 1
    for (int i = NSTEP_RUN - RING; i >= 0; i -= RING) {
#pragma unroll
      for (int r = 0; r < RING; ++r) {
        unsigned lv = *(volatile LAS unsigned*)(L + L_LVL);
        *(volatile LAS unsigned*)(L + L_PROG + 4 * wave) = (unsigned)(NSTEP - RING + r - i);
        const unsigned pp = scores_to_p(ka[r], kb[r], qf, a);
        lv = i ? uni(lv) : 8u;
        pace_lvl(lv);
        ka[r] = __builtin_amdgcn_raw_buffer_load_b128(rk, ko, 0, NT);
        kb[r] = __builtin_amdgcn_raw_buffer_load_b128(rk, ko + 1024, 0, NT);
        if (r == RING - 1) *(volatile LAS f2*)(L + a_ml) = f2{a.m, rows_add(a.l)};
        __builtin_amdgcn_sched_barrier(0);
        pv(va[r], vb[r], pp, sel, a);
        pace_lvl(lv);
        va[r] = __builtin_amdgcn_raw_buffer_load_b128(rv, vo, 0, NT);
        vb[r] = __builtin_amdgcn_raw_buffer_load_b128(rv, vo + 1024, 0, NT);
        ko += STRIDE; vo += STRIDE;
        __builtin_amdgcn_sched_barrier(0);
      }
    }
    STAMP(2);
#pragma unroll
    for (int i = 0; i < 4; ++i) { *(LAS float*)(L + a_ow + 32 * i) = a.o0[i]; *(LAS float*)(L + a_ow + 32 * i + 16) = a.o1[i]; }
    lds_barrier();
    {
      const f4 ml0 = *(const LAS f4*)(L + L_ML + wave * 32), ml1 = *(const LAS f4*)(L + L_ML + wave * 32 + 16);
      const f2 t0 = *(const LAS f2*)(L + a_or), t1 = *(const LAS f2*)(L + a_or + REC), t2 = *(const LAS f2*)(L + a_or + 2 * REC), t3 = *(const LAS f2*)(L + a_or + 3 * REC);
      __builtin_amdgcn_sched_barrier(0);
      const float M = fmaxf(fmaxf(ml0.x, ml0.z), fmaxf(ml1.x, ml1.z));
      const float f0 = ex2((ml0.x - M) * LOG2E), f1 = ex2((ml0.z - M) * LOG2E), f2_ = ex2((ml1.x - M) * LOG2E), f3 = ex2((ml1.z - M) * LOG2E);
      const f2 o = (f0 * t0 + f1 * t1) + (f2_ * t2 + f3 * t3);
      const float ls = fmaf(f0, ml0.y, f1 * ml0.w) + fmaf(f2_, ml1.y, f3 * ml1.w);
      put3(rs, a_po, asu(o.x), asu(o.y), asu((lane & 1) ? ls : M), e);
    }
    STAMP(3);
    if (wave == 0) { FLUSH(0, 3); FLUSH(14, 14); }
    if (s >= 8) return;
    int off[4];
#pragma unroll
    for (int i = 0; i < 4; ++i) { off[i] = WS_A + (64 * wave + 16 * i + (lane >> 2)) * ROW + (4 * id + c) * 16; pin(off[i]); }
    GAS unsigned short* po = (GAS unsigned short*)p.out + 2 * (16 * (id & 15) + 4 * c + (id >> 4)) + ((lane >> 2) & 1);
    int a_fw = L_FIN + wave * 32 + (lane >> 5) * 16 + c * 4;
    pin(po); pin(a_fw);
    const unsigned t_end = uni(*(volatile LAS unsigned*)(L + L_TEND));
    wait_word<2>(L + L_FLAG);
    if (D2) __builtin_amdgcn_s_sleep(D2);
    if (T2) wait_until(t_end + T2);
    v4u t[4];
    gather<4, false>(rs, off, e, t);
    STAMP(10);
    const float av = reduce_quads((asf(t[0].x) + asf(t[1].x)) + (asf(t[2].x) + asf(t[3].x)), (asf(t[0].y) + asf(t[1].y)) + (asf(t[2].y) + asf(t[3].y)));
    if ((lane & 28) == 0) *(LAS float*)(L + a_fw) = av;
    lds_barrier();
    if (wave == 0 && lane < 8) {
      const LAS float* fin = (const LAS float*)(L + L_FIN) + lane;
      const f2 bx = *(const LAS f2*)(L + L_B + lane * 8);
      const float r = rbf((fin[0] + fin[8]) + (fin[16] + fin[24]));
      *po = (unsigned short)pk(rbf(bx.y + r) + bx.x, 0.f);
      if (b == 0 && lane == 0) __builtin_amdgcn_raw_buffer_store_b64(e, rs, WS_EPOCH, 0, SC1);
    }
    STAMP(11);
    if (wave == 0) FLUSH(10, 11);
    return;
  }

  const int rho = lane >> 3, c = lane & 7;
  if (wave == 4) {
    const int kq = s & 3, strip = s >> 2;
    const int r0 = kq * 128 + 16 * rho;
    const v4u xa = *(const GAS v4u*)(p.x + 8 * lane);
    const v4u xb0 = *(const GAS v4u*)(p.x + r0), xb1 = *(const GAS v4u*)(p.x + r0 + 8);
    const v2u eo = __builtin_amdgcn_raw_buffer_load_b64(rs, WS_EPOCH, 0, SC1);
    const unsigned short* wp = p.wq + (size_t)r0 * NQ + gx * 512 + strip * 64 + c * 8;
    v4u w[16];
#pragma unroll
    for (int i = 0; i < 16; ++i) w[i] = __builtin_nontemporal_load((const GAS v4u*)(wp + (size_t)i * NQ));
    __builtin_amdgcn_sched_barrier(0);
    __builtin_amdgcn_s_barrier();
    STAMP(12);
    float mean, rden;
    ln_stats(xa, mean, rden);
    const v4u n0 = ln_apply(xb0, mean, rden), n1 = ln_apply(xb1, mean, rden);
    v2u e = {uni(eo.x) + 1u, uni(eo.y) + 1u};
    if (e.x == 0u) e.x = 1u;
    if (e.y == 0u) e.y = 1u;
    float acc[8] = {0.f, 0.f, 0.f, 0.f, 0.f, 0.f, 0.f, 0.f};
#pragma unroll
    for (int i = 0; i < 4; ++i) mac(pair_rows(w[2 * i], w[2 * i + 1]), n0[i], acc);
#pragma unroll
    for (int i = 0; i < 4; ++i) mac(pair_rows(w[8 + 2 * i], w[9 + 2 * i]), n1[i], acc);
    const f2 u = reduce8(acc);
    if ((rho & 1) == 0) put2(rs, WS_Q + (gx * 4 + kq) * 4096 + (strip * 32 + (lane >> 4) * 8 + c) * 16, asu(u.x), asu(u.y), e);
    STAMP(4);
    int off[16];
#pragma unroll
    for (int i = 0; i < 16; ++i) off[i] = WS_Q + (g * 4 + (i & 3)) * 4096 + ((i >> 2) * 64 + lane) * 16;
    v4u q[16];
    gather<16, false>(rs, off, e, q);
#pragma unroll
    for (int i = 0; i < 4; ++i)
      *(LAS unsigned*)(L + L_Q + (i * 64 + (lane & 32) + 4 * c + (rho & 3)) * 4) =
          pk((asf(q[4 * i].x) + asf(q[4 * i + 1].x)) + (asf(q[4 * i + 2].x) + asf(q[4 * i + 3].x)),
             (asf(q[4 * i].y) + asf(q[4 * i + 1].y)) + (asf(q[4 * i + 2].y) + asf(q[4 * i + 3].y)));
    if (lane == 0) *(LAS v2u*)(L + L_E) = e;
    asm volatile("s_waitcnt lgkmcnt(0)" ::: "memory");
    if (lane == 0) *(volatile LAS unsigned*)(L + L_FLAG) = 1u;
    STAMP(6);
    FLUSH(4, 4); FLUSH(6, 6); FLUSH(12, 12);
    if (ADAPT) {
      const bool rd = lane < 8 && lane != g;
      unsigned last = 0, t_ref = 0, ref = 0, t_first = 0, ref_first = 0;
      float drift = 0.f;
      int nref = 0;
#pragma unroll 1
      for (int it = 0; it < 8192; ++it) {
        const bool exch = it % NEXCH == 0 && last < XSTOP;
        v4u t = {0u, 0u, 0u, 0u};
        asm volatile("" ::: "memory");
        if (exch && it && rd) t = __builtin_amdgcn_raw_buffer_load_b128(rs, WS_PL + (((s + 5 * (it / NEXCH) + 3 * lane) & (31 & ~PUBM)) * 8 + lane) * 128, 0, SC1);
        asm volatile("" : "+v"(t) :: "memory");
        last = progress(L);
        const unsigned now = clock32(), lag = now - (last * TAU16 >> 4);
        if (exch) {
          if (lane == 0 && (s & PUBM) == 0) put2(rs, WS_PL + b * 128, lag, now, e);
          const bool ok = rd && (t.x ^ t.z) == e.x && (t.y ^ t.w) == e.y;
          float sl = ok ? (float)(int)(t.x - lag) : 0.f, st = ok ? (float)(int)(t.y - now) : 0.f;
          sl += dpp<X1>(sl); st += dpp<X1>(st); sl += dpp<X2>(sl); st += dpp<X2>(st); sl += dpp<X7>(sl); st += dpp<X7>(st);
          const int n = __builtin_popcountll(__builtin_amdgcn_ballot_w64(ok));
          if (n >= 4) {
            const float w = rcp((float)(n + 1));
            ref = lag + uni((unsigned)(int)(sl * w));
            t_ref = now + uni((unsigned)(int)(st * w));
            if (nref == 1) { ref_first = ref; t_first = t_ref; }
            else if (nref > 1) drift = (float)(int)(ref - ref_first) * rcp((float)(int)(t_ref - t_first) + 200.f);
            ++nref;
          }
        }
        const int lead = nref ? (int)(ref - lag + uni((unsigned)(int)(drift * (float)(int)(now - t_ref)))) : 0;
        if (last >= 4 * (NSTEP - RING)) {
          if (lane == 0) *(volatile LAS unsigned*)(L + L_TEND) = lag + (unsigned)lead + (4 * (NSTEP - RING) * TAU16 >> 4);
          break;
        }
        if (lane == 0) *(volatile LAS unsigned*)(L + L_LVL) = (unsigned)min(max((UNOM - lead * KP) >> 8, 0), 7);
        __builtin_amdgcn_s_sleep(CSLEEP);
      }
      asm volatile("s_waitcnt lgkmcnt(0)" ::: "memory");
    }
    return;
  }

  if (wave == 5) {
    const v4u cpf = __builtin_amdgcn_raw_buffer_load_b128(
        __builtin_amdgcn_make_buffer_rsrc((void*)code, 0, code_len > 0 && code_len <= 32768 ? code_len & ~15 : 0, 0x00020000), s * 1024 + lane * 16, 0, 0);
    __builtin_amdgcn_sched_barrier(0);
    __builtin_amdgcn_s_barrier();
    asm volatile("" :: "v"(cpf));
    wait_word<4>(L + L_CNT);
    Pairs w[8];
#pragma unroll
    for (int j = 0; j < 8; ++j) {
      w[j] = pair_rows(*(const LAS v4u*)(L + L_WO + 2 * j * 1024 + lane * 16), *(const LAS v4u*)(L + L_WO + (2 * j + 1) * 1024 + lane * 16));
#pragma unroll
      for (int k = 0; k < 8; ++k) pin(w[j].c[k]);
    }
    int off[4], a_pa = WS_A + (g * 32 + s) * ROW + lane * 16;
#pragma unroll
    for (int i = 0; i < 4; ++i) { off[i] = WS_O + (gx * 32 + rho + 8 * i) * ROW + s * 128 + c * 16; pin(off[i]); }
    pin(a_pa);
    lds_barrier();
    const v2u e = *(const LAS v2u*)(L + L_E);
    const unsigned t_end = uni(*(volatile LAS unsigned*)(L + L_TEND));
    STAMP(16);
    if (D1) __builtin_amdgcn_s_sleep(D1);
    if (T1) wait_until(t_end + T1);
    v4u t[4];
    gather<4, true>(rs, off, e, t);
    STAMP(8);
    const float m0 = dpp<0xA0>(asf(t[0].z)), m1 = dpp<0xA0>(asf(t[1].z)), m2 = dpp<0xA0>(asf(t[2].z)), m3 = dpp<0xA0>(asf(t[3].z));
    float M = fmaxf(fmaxf(m0, m1), fmaxf(m2, m3));
    M = rows_max(fmaxf(M, dpp<R8>(M)));
    const float f0 = ex2((m0 - M) * LOG2E), f1 = ex2((m1 - M) * LOG2E), f2_ = ex2((m2 - M) * LOG2E), f3 = ex2((m3 - M) * LOG2E);
    float o0 = fmaf(f0, asf(t[0].x), f1 * asf(t[1].x)) + fmaf(f2_, asf(t[2].x), f3 * asf(t[3].x));
    float o1 = fmaf(f0, asf(t[0].y), f1 * asf(t[1].y)) + fmaf(f2_, asf(t[2].y), f3 * asf(t[3].y));
    float ls = dpp<0xF5>(fmaf(f0, asf(t[0].z), f1 * asf(t[1].z)) + fmaf(f2_, asf(t[2].z), f3 * asf(t[3].z)));
    o0 += dpp<R8>(o0); o1 += dpp<R8>(o1); ls += dpp<R8>(ls);
    o0 = rows_add(o0); o1 = rows_add(o1); ls = rcp(rows_add(ls));
    const unsigned op = pk(o0 * ls, o1 * ls);
    unsigned oj[8];
#pragma unroll
    for (int j = 0; j < 8; ++j) oj[j] = __builtin_amdgcn_readlane(op, j);
    __builtin_amdgcn_sched_barrier(0);
    float acc[8] = {0.f, 0.f, 0.f, 0.f, 0.f, 0.f, 0.f, 0.f};
#pragma unroll
    for (int j = 0; j < 8; ++j) mac(w[j], oj[j], acc);
#pragma unroll
    for (int k = 0; k < 4; ++k) put2(rs, a_pa + k * 1024, asu(acc[2 * k]), asu(acc[2 * k + 1]), e);
    if (lane == 0) *(volatile LAS unsigned*)(L + L_FLAG) = 2u;
    STAMP(9);
    FLUSH(8, 9); FLUSH(16, 17);
    return;
  }

  __builtin_amdgcn_s_barrier();
  if (s >= NFFN) return;
  const int id = (s & 7) * 8 + g;
  const v4u xa = *(const GAS v4u*)(p.x + 8 * lane);
  const v4u wr = *(const GAS v4u*)(p.wr + 8 * lane);
  const unsigned xx = *(const GAS unsigned*)(p.x + 2 * (16 * (id & 15) + 4 * (lane & 3) + (id >> 4)));
  const v4u xb = *(const GAS v4u*)(p.x + g * 64 + 8 * rho);
  float mean, rden;
  ln_stats(xa, mean, rden);
  wait_word<4>(L + L_CNT);
  Pairs wd[4];
  unsigned gu0, gu1;
  {
    const v4u n = ln_apply(xb, mean, rden);
    float ga[8] = {0.f, 0.f, 0.f, 0.f, 0.f, 0.f, 0.f, 0.f}, ua[8] = {0.f, 0.f, 0.f, 0.f, 0.f, 0.f, 0.f, 0.f};
#pragma unroll
    for (int i = 0; i < 4; ++i) {
      const LAS unsigned char* w = L + 2 * i * 1024 + lane * 16;
      mac(pair_rows(*(const LAS v4u*)(w + L_WG), *(const LAS v4u*)(w + L_WG + 1024)), n[i], ga);
      mac(pair_rows(*(const LAS v4u*)(w + L_WU), *(const LAS v4u*)(w + L_WU + 1024)), n[i], ua);
      wd[i] = pair_rows(*(const LAS v4u*)(w + L_WD), *(const LAS v4u*)(w + L_WD + 1024));
#pragma unroll
      for (int k = 0; k < 8; ++k) pin(wd[i].c[k]);
    }
    const f2 gs = reduce8(ga), us = reduce8(ua);
    gu0 = asu((rho & 1) ? us.x : gs.x); gu1 = asu((rho & 1) ? us.y : gs.y);
  }
  const v4u nx = ln_apply(xa, mean, rden);
  const float r = rbf(wave_sum(dot2(nx.x, wr.x, dot2(nx.y, wr.y, 0.f)) + dot2(nx.z, wr.z, dot2(nx.w, wr.w, 0.f))));
  float sg = rbf(rcp(1.0f + ex2(-LOG2E * r)));
  const unsigned xnp = ln_pair(xx, mean, rden);
  float xn1 = lane < 32 ? lo(xnp) : hi(xnp), x1 = lane < 32 ? lo(xx) : hi(xx);
  int og[8], oy[2], a_py = WS_Y + s * ROW + (g * 4 + (lane >> 4)) * 128 + c * 16;
#pragma unroll
  for (int i = 0; i < 8; ++i) og[i] = WS_GU + (s * 8 + i) * 1024 + lane * 16;
  const bool two = (lane >> 2) + 16 < NFFN;
  oy[0] = WS_Y + (((id & 15) >> 1) * 4 + (id >> 4)) * 128 + (4 * (id & 1) + (lane & 3)) * 16 + (lane >> 2) * ROW;
  oy[1] = oy[0] + (two ? 16 * ROW : 0);
  pin(gu0); pin(gu1); pin(sg); pin(xn1); pin(x1); pin(oy[0]); pin(oy[1]); pin(a_py);
  wait_word<1>(L + L_FLAG);
  const v2u e = *(const LAS v2u*)(L + L_E);
#pragma unroll 1
  for (int it = 0; it < 65536 && progress(L) < GPROG; ++it) __builtin_amdgcn_s_sleep(4);
  put2(rs, WS_GU + (s * 8 + g) * 1024 + lane * 16, gu0, gu1, e);
  lds_barrier();
  v4u gu[8];
  gather<8, false>(rs, og, e, gu);
  const unsigned ap = pk(((asf(gu[0].x) + asf(gu[1].x)) + (asf(gu[2].x) + asf(gu[3].x))) + ((asf(gu[4].x) + asf(gu[5].x)) + (asf(gu[6].x) + asf(gu[7].x))),
                         ((asf(gu[0].y) + asf(gu[1].y)) + (asf(gu[2].y) + asf(gu[3].y))) + ((asf(gu[4].y) + asf(gu[5].y)) + (asf(gu[6].y) + asf(gu[7].y))));
  const float a0 = lo(ap), a1 = hi(ap);
  const unsigned sp = pk(a0 * rcp(1.0f + ex2(-LOG2E * a0)), a1 * rcp(1.0f + ex2(-LOG2E * a1)));
  const int hp = (int)pk(lo(sp) * dpp<R8>(a0), hi(sp) * dpp<R8>(a1));
  float y[8] = {0.f, 0.f, 0.f, 0.f, 0.f, 0.f, 0.f, 0.f};
#pragma unroll
  for (int i = 0; i < 4; ++i) mac(wd[i], (unsigned)__builtin_amdgcn_ds_bpermute((16 * i + rho) * 4, hp), y);
  const f2 ys = reduce8(y);
  if ((rho & 1) == 0) put2(rs, a_py, asu(ys.x), asu(ys.y), e);
  STAMP(5);
  FLUSH(5, 5);
  if (s >= 8) return;
  __builtin_amdgcn_s_sleep(DY);
  v4u yv[2];
  gather<2, false>(rs, oy, e, yv);
  const float yc = rbf(reduce_quads(asf(yv[0].x) + (two ? asf(yv[1].x) : 0.f), asf(yv[0].y) + (two ? asf(yv[1].y) : 0.f)));
  if ((lane & 28) == 0) *(LAS f2*)(L + L_B + ((lane >> 5) * 4 + (lane & 3)) * 8) = f2{rbf(xn1 + rbf(yc * sg)), x1};
  asm volatile("s_waitcnt lgkmcnt(0)" ::: "memory");
  STAMP(7);
  FLUSH(7, 7);
}

extern "C" __device__ __attribute__((used, noinline)) void END_MARK() {}

extern "C" void kernel_launch(void* const* d_in, const int* in_sizes, int n_in, void* d_out, int out_size, void* d_ws, size_t ws_size,
                              hipStream_t stream) {
  (void)in_sizes; (void)n_in; (void)out_size;
  Params p;
  p.x = (const unsigned short*)d_in[0];
  p.k = (const unsigned short*)d_in[1];
  p.v = (const unsigned short*)d_in[2];
  p.wq = (const unsigned short*)d_in[3];
  p.wo = (const unsigned short*)d_in[4];
  p.wr = (const unsigned short*)d_in[6];
  p.wg = (const unsigned short*)d_in[7];
  p.wu = (const unsigned short*)d_in[8];
  p.wd = (const unsigned short*)d_in[9];
  p.out = (unsigned*)d_out;
  p.ws = (unsigned char*)d_ws + (WSTAIL ? (ws_size - WS_SPAN) / 4096 * 4096 : 0);
  hipLaunchKernelGGL(KERNEL_NAME, dim3(NWG), dim3(NTHR), 0, stream, p);
}
```

```cpp
#include <hip/hip_runtime.h>
#include <hip/hip_bf16.h>
#include <stdint.h>

#define GAS __attribute__((address_space(1)))
#define LAS __attribute__((address_space(3)))
typedef unsigned v4u __attribute__((ext_vector_type(4)));
typedef unsigned v2u __attribute__((ext_vector_type(2)));
typedef unsigned v16u __attribute__((ext_vector_type(16)));
typedef float f4 __attribute__((ext_vector_type(4)));
typedef float f2 __attribute__((ext_vector_type(2)));
typedef short s16x4 __attribute__((ext_vector_type(4)));
typedef __bf16 bf16x8 __attribute__((ext_vector_type(8)));
typedef __bf16 bf16x2 __attribute__((ext_vector_type(2)));

namespace {

constexpr int D = 512, DH = 128, SK = 65536, NQ = 4096, FF = 1792;
constexpr int NWG = 256, NTHR = 448;
constexpr int KEYS_PER_WG = SK / 32;
constexpr int NSTEP = KEYS_PER_WG / 32;
constexpr int RING = 4;
constexpr int NFFN = FF / 64;
constexpr float SCALE = 0.08838834764831845f, LOG2E = 1.4426950408889634f, FMIN = -3.4028234664e38f;
constexpr int PACE0 = 4, GAIN = 7, UNOM = 540, NEXCH = 7, D1 = 18, D2 = 16, DY = 10;
constexpr unsigned SPIN_CAP = 1u << 16;
constexpr int TAU16 = 220, XSTOP = 226;

constexpr int ROW = 4096 + 128;
constexpr int WS_EPOCH = 0;
constexpr int WS_Q = 4096;
constexpr int WS_GU = WS_Q + 8 * 4 * 4096;
constexpr int WS_Y = WS_GU + NFFN * 8192;
constexpr int WS_O = WS_Y + NFFN * ROW;
constexpr int WS_A = WS_O + 256 * ROW;
constexpr int WS_PL = WS_A + 256 * ROW;
constexpr int WS_END = WS_PL + 32 * 128;
constexpr size_t WS_SPAN = 4u << 20;
static_assert(WS_END + 65536 <= WS_SPAN, "workspace map");

constexpr int L_Q = 0, L_E = 1024, L_FLAG = 1032, L_CNT = 1036, L_PROG = 1040, L_LAG = 1056, L_REF = 1072, L_B = 1088, L_FIN = 1152;
constexpr int L_ML = 1280;
constexpr int REC = 4 * 132 * 4, L_REC = 1536;
constexpr int L_WO = 16384, L_WG = 32768, L_WU = 40960, L_WD = 49152;
constexpr int L_SINK = 57344;
constexpr int LDS_BYTES = 84 * 1024;

struct Params {
  const unsigned short *x, *k, *v, *wq, *wo, *wr, *wg, *wu, *wd;
  unsigned* out;
  unsigned char* ws;
};

__device__ __forceinline__ float asf(unsigned u) { return __builtin_bit_cast(float, u); }
__device__ __forceinline__ unsigned asu(float f) { return __builtin_bit_cast(unsigned, f); }
__device__ __forceinline__ float lo(unsigned u) { return asf(u << 16); }
__device__ __forceinline__ float hi(unsigned u) { return asf(u & 0xffff0000u); }
__device__ __forceinline__ unsigned pk(float a, float b) {
  bf16x2 r = {(__bf16)a, (__bf16)b};
  return __builtin_bit_cast(unsigned, r);
}
__device__ __forceinline__ float rbf(float a) { return lo(pk(a, a)); }
__device__ __forceinline__ float rcp(float a) { return __builtin_amdgcn_rcpf(a); }
__device__ __forceinline__ float ex2(float a) { return __builtin_amdgcn_exp2f(a); }
__device__ __forceinline__ unsigned xor3(unsigned a, unsigned b, unsigned c) { return __builtin_amdgcn_bitop3_b32(a, b, c, 0x96); }
__device__ __forceinline__ unsigned or3(unsigned a, unsigned b, unsigned c) { return __builtin_amdgcn_bitop3_b32(a, b, c, 0xFE); }
__device__ __forceinline__ unsigned uni(unsigned a) { return (unsigned)__builtin_amdgcn_readfirstlane((int)a); }
__device__ __forceinline__ unsigned clock32() { return (unsigned)__builtin_amdgcn_s_memrealtime(); }
__device__ __forceinline__ float dot2(unsigned a, unsigned b, float c) {
  return __builtin_amdgcn_fdot2_f32_bf16(__builtin_bit_cast(bf16x2, a), __builtin_bit_cast(bf16x2, b), c, false);
}
template <class T> __device__ __forceinline__ void pin(T& x) { asm volatile("" : "+v"(x)); }
template <int CTRL> __device__ __forceinline__ float dpp(float x) {
  return asf(__builtin_amdgcn_mov_dpp(__builtin_bit_cast(int, x), CTRL, 0xf, 0xf, true));
}
constexpr int X1 = 0xB1, X2 = 0x4E, X7 = 0x141, R4 = 0x124, R8 = 0x128;
__device__ __forceinline__ float x32_tr(float a, float b) {
  auto s = __builtin_amdgcn_permlane32_swap(asu(a), asu(b), false, false);
  return asf(s[0]) + asf(s[1]);
}
__device__ __forceinline__ float x16_tr(float a, float b) {
  auto s = __builtin_amdgcn_permlane16_swap(asu(a), asu(b), false, false);
  return asf(s[0]) + asf(s[1]);
}
__device__ __forceinline__ float rows_add(float x) { x = x16_tr(x, x); return x32_tr(x, x); }
__device__ __forceinline__ float rows_max(float x) {
  auto s = __builtin_amdgcn_permlane16_swap(asu(x), asu(x), false, false);
  x = fmaxf(asf(s[0]), asf(s[1]));
  auto t = __builtin_amdgcn_permlane32_swap(asu(x), asu(x), false, false);
  return fmaxf(asf(t[0]), asf(t[1]));
}
__device__ __forceinline__ float wave_sum(float x) {
  x += dpp<X1>(x); x += dpp<X2>(x); x += dpp<X7>(x); x += dpp<R8>(x);
  return rows_add(x);
}
__device__ __forceinline__ void lds_barrier() {
  asm volatile("s_waitcnt lgkmcnt(0)" ::: "memory");
  __builtin_amdgcn_s_barrier();
  asm volatile("" ::: "memory");
}
template <int N> __device__ __forceinline__ void pace() {
  __builtin_amdgcn_sched_barrier(0);
  asm volatile("s_waitcnt vmcnt(%0)" :: "i"(N) : "memory");
  __builtin_amdgcn_sched_barrier(0);
}
__device__ __forceinline__ void pace_lvl(unsigned lvl) {
  __builtin_amdgcn_sched_barrier(0);
  asm volatile("s_cmp_gt_u32 %0, 3\n\ts_cbranch_scc1 4f\n\ts_cmp_gt_u32 %0, 1\n\ts_cbranch_scc1 2f\n\t"
               "s_cmp_eq_u32 %0, 1\n\ts_cbranch_scc1 1f\n\ts_waitcnt vmcnt(2)\n1:\n\ts_waitcnt vmcnt(3)\n\ts_branch 9f\n"
               "2:\n\ts_cmp_eq_u32 %0, 3\n\ts_cbranch_scc1 3f\n\ts_waitcnt vmcnt(4)\n3:\n\ts_waitcnt vmcnt(5)\n\ts_branch 9f\n"
               "4:\n\ts_cmp_gt_u32 %0, 5\n\ts_cbranch_scc1 6f\n\t"
               "s_cmp_eq_u32 %0, 5\n\ts_cbranch_scc1 5f\n\ts_waitcnt vmcnt(6)\n5:\n\ts_waitcnt vmcnt(7)\n\ts_branch 9f\n"
               "6:\n\ts_cmp_gt_u32 %0, 7\n\ts_cbranch_scc1 9f\n\t"
               "s_cmp_eq_u32 %0, 7\n\ts_cbranch_scc1 7f\n\ts_waitcnt vmcnt(8)\n7:\n\ts_waitcnt vmcnt(10)\n9:"
               :: "s"(lvl) : "scc");
  __builtin_amdgcn_sched_barrier(0);
}
constexpr int SC1 = 16, NT = 2;
__device__ __forceinline__ void put2(__amdgpu_buffer_rsrc_t rs, int off, unsigned a, unsigned b, v2u e) {
  __builtin_amdgcn_raw_buffer_store_b128(v4u{a, b, a ^ e.x, b ^ e.y}, rs, off, 0, SC1);
}
__device__ __forceinline__ void put3(__amdgpu_buffer_rsrc_t rs, int off, unsigned a, unsigned b, unsigned c, v2u e) {
  __builtin_amdgcn_raw_buffer_store_b128(v4u{a, b, c, xor3(a, b, c) ^ e.x}, rs, off, 0, SC1);
}
template <int N, bool THREE> __device__ __forceinline__ void gather(__amdgpu_buffer_rsrc_t rs, const int (&off)[N], v2u e, v4u (&g)[N]) {
#pragma unroll 1
  for (unsigned spin = 0; spin < SPIN_CAP; ++spin) {
    asm volatile("" ::: "memory");
#pragma unroll
    for (int i = 0; i < N; ++i) g[i] = __builtin_amdgcn_raw_buffer_load_b128(rs, off[i], 0, SC1);
    unsigned bad = 0;
#pragma unroll
    for (int i = 0; i < N; ++i) {
      if (THREE) bad |= xor3(xor3(g[i].x, g[i].y, g[i].z), g[i].w, e.x);
      else bad = or3(xor3(g[i].x, g[i].z, e.x), xor3(g[i].y, g[i].w, e.y), bad);
    }
    if (__builtin_amdgcn_ballot_w64(bad != 0u) == 0ull) break;
  }
}
template <unsigned V> __device__ __forceinline__ void wait_word(LAS unsigned char* w) {
#pragma unroll 1
  for (unsigned spin = 0; spin < (1u << 21); ++spin) {
    if (uni(*(volatile LAS unsigned*)w) >= V) break;
    __builtin_amdgcn_s_sleep(1);
  }
  asm volatile("" ::: "memory");
}
__device__ __forceinline__ unsigned progress(LAS unsigned char* L) {
  const v4u pr = *(volatile LAS v4u*)(L + L_PROG);
  return uni(pr.x + pr.y + pr.z + pr.w);
}
__device__ __forceinline__ void dma(const void* src, unsigned dst) {
  unsigned keep;
  asm volatile("s_mov_b32 %0, m0\n\ts_mov_b32 m0, %2\n\ts_nop 0\n\tglobal_load_lds_dwordx4 %1, off nt\n\ts_mov_b32 m0, %0"
               : "=&s"(keep) : "v"(src), "s"(dst) : "memory");
}
__device__ __forceinline__ void ln_stats(v4u xa, float& mean, float& rden) {
  const unsigned one = 0x3f803f80u;
  const float mu = wave_sum(dot2(xa.x, one, dot2(xa.y, one, 0.f)) + dot2(xa.z, one, dot2(xa.w, one, 0.f))) * (1.0f / D);
  float q0 = 0.f, q1 = 0.f;
#pragma unroll
  for (int j = 0; j < 4; ++j) { const float a = lo(xa[j]) - mu, b = hi(xa[j]) - mu; q0 = fmaf(a, a, q0); q1 = fmaf(b, b, q1); }
  const float var = rbf(wave_sum(q0 + q1) * (1.0f / D));
  mean = rbf(mu);
  rden = rcp(rbf(__builtin_amdgcn_sqrtf(rbf(var + 1e-5f))));
}
__device__ __forceinline__ unsigned ln_pair(unsigned xx, float mean, float rden) {
  const unsigned d = pk(lo(xx) - mean, hi(xx) - mean);
  return pk(lo(d) * rden, hi(d) * rden);
}
__device__ __forceinline__ v4u ln_apply(v4u xa, float mean, float rden) {
  return v4u{ln_pair(xa.x, mean, rden), ln_pair(xa.y, mean, rden), ln_pair(xa.z, mean, rden), ln_pair(xa.w, mean, rden)};
}
struct Pairs { unsigned c[8]; };
__device__ __forceinline__ Pairs pair_rows(v4u w0, v4u w1) {
  Pairs r;
#pragma unroll
  for (int k = 0; k < 4; ++k) {
    r.c[2 * k] = __builtin_amdgcn_perm(w1[k], w0[k], 0x05040100u);
    r.c[2 * k + 1] = __builtin_amdgcn_perm(w1[k], w0[k], 0x07060302u);
  }
  return r;
}
__device__ __forceinline__ void mac(const Pairs& w, unsigned x, float (&acc)[8]) {
#pragma unroll
  for (int k = 0; k < 8; ++k) acc[k] = dot2(w.c[k], x, acc[k]);
}
__device__ __forceinline__ f2 reduce8(const float (&acc)[8]) {
  const float t0 = x32_tr(acc[0], acc[4]), t1 = x32_tr(acc[1], acc[5]), t2 = x32_tr(acc[2], acc[6]), t3 = x32_tr(acc[3], acc[7]);
  const float u0 = x16_tr(t0, t2), u1 = x16_tr(t1, t3);
  return f2{u0 + dpp<R8>(u0), u1 + dpp<R8>(u1)};
}
__device__ __forceinline__ float reduce_quads(float a, float b) {
  float t = x32_tr(a, b);
  t = x16_tr(t, t);
  t += dpp<R4>(t);
  return t + dpp<R8>(t);
}

struct Acc { f4 o0, o1; float m, l; };

__device__ __forceinline__ unsigned scores_to_p(v4u ka, v4u kb, bf16x8 qf, Acc& s) {
  const f4 z = {0.f, 0.f, 0.f, 0.f};
  const f4 da = __builtin_amdgcn_mfma_f32_16x16x32_bf16(__builtin_bit_cast(bf16x8, ka), qf, z, 0, 0, 0);
  const f4 db = __builtin_amdgcn_mfma_f32_16x16x32_bf16(__builtin_bit_cast(bf16x8, kb), qf, z, 0, 0, 0);
  float sa = (dpp<0x00>(da[0]) + dpp<0x55>(da[1])) + (dpp<0xAA>(da[2]) + dpp<0xFF>(da[3]));
  float sb = (dpp<0x00>(db[0]) + dpp<0x55>(db[1])) + (dpp<0xAA>(db[2]) + dpp<0xFF>(db[3]));
  unsigned u = pk(sa, sb);
  u = pk(lo(u) * SCALE, hi(u) * SCALE);
  sa = lo(u); sb = hi(u);
  float mx = fmaxf(sa, sb);
  if (__builtin_amdgcn_ballot_w64(mx > s.m) != 0ull) {
    mx = fmaxf(s.m, rows_max(mx));
    const float al = ex2((s.m - mx) * LOG2E);
    s.o0 *= al; s.o1 *= al; s.l *= al; s.m = mx;
  }
  const float pa = ex2((sa - s.m) * LOG2E), pb = ex2((sb - s.m) * LOG2E);
  s.l += pa + pb;
  return pk(pa, pb);
}
__device__ __forceinline__ void pv(v4u va, v4u vb, unsigned pp, v4u sel, Acc& s) {
  const v2u ba = {__builtin_amdgcn_perm(pp, pp, sel.x), __builtin_amdgcn_perm(pp, pp, sel.y)};
  const v2u bb = {__builtin_amdgcn_perm(pp, pp, sel.z), __builtin_amdgcn_perm(pp, pp, sel.w)};
  s.o0 = __builtin_amdgcn_mfma_f32_16x16x16bf16_1k(__builtin_bit_cast(s16x4, v2u{va.x, va.y}), __builtin_bit_cast(s16x4, ba), s.o0, 0, 0, 0);
  s.o1 = __builtin_amdgcn_mfma_f32_16x16x16bf16_1k(__builtin_bit_cast(s16x4, v2u{va.z, va.w}), __builtin_bit_cast(s16x4, ba), s.o1, 0, 0, 0);
  s.o0 = __builtin_amdgcn_mfma_f32_16x16x16bf16_1k(__builtin_bit_cast(s16x4, v2u{vb.x, vb.y}), __builtin_bit_cast(s16x4, bb), s.o0, 0, 0, 0);
  s.o1 = __builtin_amdgcn_mfma_f32_16x16x16bf16_1k(__builtin_bit_cast(s16x4, v2u{vb.z, vb.w}), __builtin_bit_cast(s16x4, bb), s.o1, 0, 0, 0);
}

}

extern "C" __global__ void __launch_bounds__(NTHR, 2) fused_decode_kernel(Params) {
  __shared__ __attribute__((aligned(16))) unsigned char smem[LDS_BYTES];
  LAS unsigned char* L = (LAS unsigned char*)smem;
  v16u arg0; v4u arg1; v2u arg2;
  int wave;
  {
    unsigned long long pc, wo;
    unsigned t0, t1, m0s, v0, v1;
    asm volatile("s_load_dwordx16 %0, %11, 0x0\n\ts_load_dwordx4 %1, %11, 0x40\n\ts_load_dwordx2 %2, %11, 0x50\n\ts_load_dwordx2 %4, %11, 0x20\n\t"
                 "v_readfirstlane_b32 %5, %12\n\tv_and_b32 %9, 63, %12\n\ts_mov_b32 %8, m0\n\ts_lshr_b32 %5, %5, 6\n\t"
                 "v_lshlrev_b32 %9, 4, %9\n\ts_lshr_b32 %6, %13, 3\n\ts_cmp_lg_u32 %5, " "5" "\n\ts_cbranch_scc1 1f\n\t"
                 ".p2align 4\n\tv_lshl_or_b32 %10, %6, 10, %9\n\ts_nop 0\n\ts_getpc_b64 %3\n\ts_mov_b32 %7, fused_decode_code_end@rel32@lo-12\n\t"
                 "s_and_b32 %7, %7, -16\n\ts_cmpk_gt_u32 %7, 0x7ff0\n\ts_cbranch_scc1 1f\n\t"
                 "s_mov_b32 m0, %14\n\tv_min_u32 %10, %7, %10\n\tglobal_load_lds_dwordx4 %10, %3\n"
                 "1:\n\ts_lshl_b32 %7, %5, 12\n\ts_add_u32 m0, %7, %15\n\ts_lshl_b32 %6, %6, 2\n\ts_add_u32 %6, %6, %5\n\t"
                 "s_and_b32 %7, %13, 7\n\ts_xor_b32 %7, %7, 1\n\ts_lshl_b32 %7, %7, 7\n\ts_add_u32 %6, %6, %7\n\t"
                 "v_lshl_or_b32 %10, %6, 12, %9\n\ts_cmp_gt_u32 %5, 3\n\ts_waitcnt lgkmcnt(0)\n\ts_cbranch_scc1 2f\n\t"
                 "global_load_lds_dwordx4 %10, %4 nt\n\tglobal_load_lds_dwordx4 %10, %4 offset:1024 nt\n\t"
                 "global_load_lds_dwordx4 %10, %4 offset:2048 nt\n\tglobal_load_lds_dwordx4 %10, %4 offset:3072 nt\n"
                 "2:\n\ts_mov_b32 m0, %8"
                 : "=&s"(arg0), "=&s"(arg1), "=&s"(arg2), "=&s"(pc), "=&s"(wo), "=&s"(wave), "=&s"(t0), "=&s"(t1), "=&s"(m0s), "=&v"(v0), "=&v"(v1)
                 : "s"(__builtin_amdgcn_kernarg_segment_ptr()), "v"(threadIdx.x), "s"(blockIdx.x), "i"(L_SINK), "i"(L_WO)
                 : "scc", "memory");
  }
  const int lane = threadIdx.x & 63;
  const int b = blockIdx.x, g = b & 7, s = b >> 3;
  Params p;
  {
    auto ptr = [](unsigned l, unsigned h) { return (const unsigned short*)(const GAS unsigned short*)(((unsigned long long)h << 32) | l); };
    p.x = ptr(arg0[0], arg0[1]); p.k = ptr(arg0[2], arg0[3]); p.v = ptr(arg0[4], arg0[5]); p.wq = ptr(arg0[6], arg0[7]);
    p.wo = ptr(arg0[8], arg0[9]); p.wr = ptr(arg0[10], arg0[11]); p.wg = ptr(arg0[12], arg0[13]); p.wu = ptr(arg0[14], arg0[15]);
    p.wd = ptr(arg1[0], arg1[1]); p.out = (unsigned*)ptr(arg1[2], arg1[3]); p.ws = (unsigned char*)ptr(arg2[0], arg2[1]);
  }
  const int gx = g ^ 1;
  const __amdgpu_buffer_rsrc_t rs = __builtin_amdgcn_make_buffer_rsrc((void*)p.ws, 0, WS_END, 0x00020000);

  if (wave < 4) {
    const size_t base = ((size_t)g * SK + (size_t)s * KEYS_PER_WG) * DH;
    const __amdgpu_buffer_rsrc_t rk = __builtin_amdgcn_make_buffer_rsrc((void*)(p.k + base), 0, KEYS_PER_WG * DH * 2, 0x00020000);
    const __amdgpu_buffer_rsrc_t rv = __builtin_amdgcn_make_buffer_rsrc((void*)(p.v + base), 0, KEYS_PER_WG * DH * 2, 0x00020000);
    const int h = (lane >> 2) & 3, c = lane & 3;
    const int frag = h * 256 + (lane >> 4) * 64 + c * 16;
    int ko = wave * 2048 + frag, vo = wave * 2048 + lane * 16;
    constexpr int STRIDE = 32 * DH * 2;
    if (threadIdx.x == 0) {
      *(volatile LAS v2u*)(L + L_FLAG) = v2u{0u, 0u};
      *(volatile LAS v4u*)(L + L_PROG) = v4u{0u, 0u, 0u, 0u};
      *(volatile LAS v2u*)(L + L_REF) = v2u{0u, 0u};
    }
    const unsigned lb = (unsigned)(size_t)L;
    const int row = 8 * (lane >> 3) + 2 * wave;
    const unsigned short* wg = p.wg + (size_t)(g * 64 + row) * FF + s * 64 + (lane & 7) * 8;
    const unsigned short* wu = p.wu + (size_t)(g * 64 + row) * FF + s * 64 + (lane & 7) * 8;
    const unsigned short* wd = p.wd + (size_t)(s * 64 + row) * D + g * 64 + (lane & 7) * 8;
    pin(wg); pin(wu); pin(wd); pin(ko); pin(vo);
    lds_barrier();
    v4u ka[RING], kb[RING], va[RING], vb[RING];
    if (s < NFFN) {
      dma(wg, lb + L_WG + 2 * wave * 1024);
      dma(wu, lb + L_WU + 2 * wave * 1024);
      pace<PACE0>();
      dma(wg + FF, lb + L_WG + (2 * wave + 1) * 1024);
      dma(wu + FF, lb + L_WU + (2 * wave + 1) * 1024);
      pace<PACE0>();
      dma(wd, lb + L_WD + 2 * wave * 1024);
      dma(wd + D, lb + L_WD + (2 * wave + 1) * 1024);
    }
#pragma unroll
    for (int r = 0; r < RING; ++r) {
      pace<PACE0>();
      if (r == (PACE0 + 3) / 4 && lane == 0)
        __hip_atomic_fetch_add((LAS unsigned*)(L + L_CNT), 1u, __ATOMIC_RELAXED, __HIP_MEMORY_SCOPE_WORKGROUP);
      ka[r] = __builtin_amdgcn_raw_buffer_load_b128(rk, ko, 0, NT);
      kb[r] = __builtin_amdgcn_raw_buffer_load_b128(rk, ko + 1024, 0, NT);
      pace<PACE0>();
      va[r] = __builtin_amdgcn_raw_buffer_load_b128(rv, vo, 0, NT);
      vb[r] = __builtin_amdgcn_raw_buffer_load_b128(rv, vo + 1024, 0, NT);
      ko += STRIDE; vo += STRIDE;
    }
    __builtin_amdgcn_sched_barrier(0);
    const unsigned z = 0x0c0c0c0cu;
    const v4u sel = {c == 0 ? 0x0c0c0100u : c == 1 ? 0x01000c0cu : z, c == 2 ? 0x0c0c0100u : c == 3 ? 0x01000c0cu : z,
                     c == 0 ? 0x0c0c0302u : c == 1 ? 0x03020c0cu : z, c == 2 ? 0x0c0c0302u : c == 3 ? 0x03020c0cu : z};
    int a_ml = L_ML + h * 32 + wave * 8, a_ow = L_REC + wave * REC + (h * 132 + (lane >> 4) * 32 + c) * 4, a_or = L_REC + (wave * 132 + 2 * lane) * 4;
    int a_po = WS_O + (g * 32 + s) * ROW + threadIdx.x * 16;
    pin(a_ml); pin(a_ow); pin(a_or); pin(a_po);
    wait_word<1>(L + L_FLAG);
    const bf16x8 qf = __builtin_bit_cast(bf16x8, *(const LAS v4u*)(L + L_Q + frag));
    v2u e = *(const LAS v2u*)(L + L_E);
    pin(e);
    Acc a;
    a.o0 = f4{0.f, 0.f, 0.f, 0.f}; a.o1 = a.o0; a.m = FMIN; a.l = 0.f;
#pragma unroll 1
    for (int i = NSTEP - RING; i >= 0; i -= RING) {
#pragma unroll
      for (int r = 0; r < RING; ++r) {
        const int n = NSTEP - RING + r - i;
        const v2u ref = *(volatile LAS v2u*)(L + L_REF);
        const unsigned lag = clock32() - (n * (4 * TAU16) >> 4);
        *(volatile LAS unsigned*)(L + L_PROG + 4 * wave) = (unsigned)n;
        *(volatile LAS unsigned*)(L + L_LAG + 4 * wave) = lag;
        const unsigned pp = scores_to_p(ka[r], kb[r], qf, a);
        int lv = (UNOM - (int)((uni(ref.x) - lag) & uni(ref.y)) * GAIN) >> 8;
        asm("s_max_i32 %0, %0, 0\n\ts_min_i32 %0, %0, 7" : "+s"(lv) :: "scc");
        lv = i ? lv : 8;
        pace_lvl(lv);
        ka[r] = __builtin_amdgcn_raw_buffer_load_b128(rk, ko, 0, NT);
        kb[r] = __builtin_amdgcn_raw_buffer_load_b128(rk, ko + 1024, 0, NT);
        if (r == RING - 1) *(volatile LAS f2*)(L + a_ml) = f2{a.m, rows_add(a.l)};
        __builtin_amdgcn_sched_barrier(0);
        pv(va[r], vb[r], pp, sel, a);
        pace_lvl(lv);
        va[r] = __builtin_amdgcn_raw_buffer_load_b128(rv, vo, 0, NT);
        vb[r] = __builtin_amdgcn_raw_buffer_load_b128(rv, vo + 1024, 0, NT);
        ko += STRIDE; vo += STRIDE;
        __builtin_amdgcn_sched_barrier(0);
      }
    }
#pragma unroll
    for (int i = 0; i < 4; ++i) { *(LAS float*)(L + a_ow + 32 * i) = a.o0[i]; *(LAS float*)(L + a_ow + 32 * i + 16) = a.o1[i]; }
    lds_barrier();
    {
      const f4 ml0 = *(const LAS f4*)(L + L_ML + wave * 32), ml1 = *(const LAS f4*)(L + L_ML + wave * 32 + 16);
      const f2 t0 = *(const LAS f2*)(L + a_or), t1 = *(const LAS f2*)(L + a_or + REC), t2 = *(const LAS f2*)(L + a_or + 2 * REC), t3 = *(const LAS f2*)(L + a_or + 3 * REC);
      __builtin_amdgcn_sched_barrier(0);
      const float M = fmaxf(fmaxf(ml0.x, ml0.z), fmaxf(ml1.x, ml1.z));
      const float f0 = ex2((ml0.x - M) * LOG2E), f1 = ex2((ml0.z - M) * LOG2E), f2_ = ex2((ml1.x - M) * LOG2E), f3 = ex2((ml1.z - M) * LOG2E);
      const f2 o = (f0 * t0 + f1 * t1) + (f2_ * t2 + f3 * t3);
      const float ls = fmaf(f0, ml0.y, f1 * ml0.w) + fmaf(f2_, ml1.y, f3 * ml1.w);
      put3(rs, a_po, asu(o.x), asu(o.y), asu((lane & 1) ? ls : M), e);
    }
    if (s >= 8) return;
    const int sl = 4 * (g * 8 + s) + c;
    int off[4];
#pragma unroll
    for (int i = 0; i < 4; ++i) { off[i] = WS_A + (64 * wave + 16 * i + (lane >> 2)) * ROW + sl * 16; pin(off[i]); }
    GAS unsigned short* po = (GAS unsigned short*)p.out + 2 * (4 * (sl & 63) + (sl >> 6)) + ((lane >> 2) & 1);
    int a_fw = L_FIN + wave * 32 + (lane >> 5) * 16 + c * 4;
    pin(po); pin(a_fw);
    wait_word<2>(L + L_FLAG);
    __builtin_amdgcn_s_sleep(D2);
    v4u t[4];
    gather<4, false>(rs, off, e, t);
    const float av = reduce_quads((asf(t[0].x) + asf(t[1].x)) + (asf(t[2].x) + asf(t[3].x)), (asf(t[0].y) + asf(t[1].y)) + (asf(t[2].y) + asf(t[3].y)));
    if ((lane & 28) == 0) *(LAS float*)(L + a_fw) = av;
    lds_barrier();
    if (wave == 0 && lane < 8) {
      const LAS float* fin = (const LAS float*)(L + L_FIN) + lane;
      const f2 bx = *(const LAS f2*)(L + L_B + lane * 8);
      const float r = rbf((fin[0] + fin[8]) + (fin[16] + fin[24]));
      *po = (unsigned short)pk(rbf(bx.y + r) + bx.x, 0.f);
      if (b == 0 && lane == 0) __builtin_amdgcn_raw_buffer_store_b64(e, rs, WS_EPOCH, 0, SC1);
    }
    return;
  }

  const int rho = lane >> 3, c = lane & 7;
  if (wave == 4) {
    const int kq = s & 3, strip = s >> 2;
    const int r0 = kq * 128 + 16 * rho;
    const v4u xa = *(const GAS v4u*)(p.x + 8 * lane);
    const v4u xb0 = *(const GAS v4u*)(p.x + r0), xb1 = *(const GAS v4u*)(p.x + r0 + 8);
    const v2u eo = __builtin_amdgcn_raw_buffer_load_b64(rs, WS_EPOCH, 0, SC1);
    const __amdgpu_buffer_rsrc_t rq = __builtin_amdgcn_make_buffer_rsrc((void*)p.wq, 0, D * NQ * 2, 0x00020000);
    const int qo = (r0 * NQ + gx * 512 + strip * 64 + c * 8) * 2;
    v4u w[16];
#pragma unroll
    for (int i = 0; i < 16; ++i) w[i] = __builtin_amdgcn_raw_buffer_load_b128(rq, qo, i * NQ * 2, NT);
    __builtin_amdgcn_sched_barrier(0);
    __builtin_amdgcn_s_barrier();
    float mean, rden;
    ln_stats(xa, mean, rden);
    const v4u n0 = ln_apply(xb0, mean, rden), n1 = ln_apply(xb1, mean, rden);
    v2u e = {uni(eo.x) + 1u, uni(eo.y) + 1u};
    if (e.x == 0u) e.x = 1u;
    if (e.y == 0u) e.y = 1u;
    float acc[8] = {0.f, 0.f, 0.f, 0.f, 0.f, 0.f, 0.f, 0.f};
#pragma unroll
    for (int i = 0; i < 4; ++i) mac(pair_rows(w[2 * i], w[2 * i + 1]), n0[i], acc);
#pragma unroll
    for (int i = 0; i < 4; ++i) mac(pair_rows(w[8 + 2 * i], w[9 + 2 * i]), n1[i], acc);
    const f2 u = reduce8(acc);
    if ((rho & 1) == 0) put2(rs, WS_Q + (gx * 4 + kq) * 4096 + (strip * 32 + (lane >> 4) * 8 + c) * 16, asu(u.x), asu(u.y), e);
    int off[16];
#pragma unroll
    for (int i = 0; i < 16; ++i) off[i] = WS_Q + (g * 4 + (i & 3)) * 4096 + ((i >> 2) * 64 + lane) * 16;
    v4u q[16];
    gather<16, false>(rs, off, e, q);
#pragma unroll
    for (int i = 0; i < 4; ++i)
      *(LAS unsigned*)(L + L_Q + (i * 64 + (lane & 32) + 4 * c + (rho & 3)) * 4) =
          pk((asf(q[4 * i].x) + asf(q[4 * i + 1].x)) + (asf(q[4 * i + 2].x) + asf(q[4 * i + 3].x)),
             (asf(q[4 * i].y) + asf(q[4 * i + 1].y)) + (asf(q[4 * i + 2].y) + asf(q[4 * i + 3].y)));
    if (lane == 0) *(LAS v2u*)(L + L_E) = e;
    asm volatile("s_waitcnt lgkmcnt(0)" ::: "memory");
    if (lane == 0) *(volatile LAS unsigned*)(L + L_FLAG) = 1u;
    const bool rd = lane < 8 && lane != g;
    unsigned last = 0, t_ref = 0, ref = 0, t_first = 0, ref_first = 0;
    float drift = 0.f;
    int nref = 0;
#pragma unroll 1
    for (int it = 0; it < 8192 && last < 4 * (NSTEP - RING); ++it) {
      const bool exch = it % NEXCH == 0 && last < XSTOP;
      v4u t = {0u, 0u, 0u, 0u};
      asm volatile("" ::: "memory");
      if (exch && it && rd) t = __builtin_amdgcn_raw_buffer_load_b128(rs, WS_PL + (((s + 5 * (it / NEXCH) + 3 * lane) & 24) + lane) * 128, 0, SC1);
      asm volatile("" : "+v"(t) :: "memory");
      const v4u pr = *(volatile LAS v4u*)(L + L_PROG), lg = *(volatile LAS v4u*)(L + L_LAG);
      const unsigned now = clock32();
      last = uni(pr.x + pr.y + pr.z + pr.w);
      const bool begun = uni(min(min(pr.x, pr.y), min(pr.z, pr.w))) != 0u;
      const unsigned lag = begun ? uni(lg.x + (unsigned)(((int)(lg.y - lg.x) + (int)(lg.z - lg.x) + (int)(lg.w - lg.x)) >> 2)) : now - (last * TAU16 >> 4);
      if (exch) {
        if (lane == 0 && (s & 7) == 0) put2(rs, WS_PL + (s + g) * 128, lag, now, e);
        const bool ok = rd && (t.x ^ t.z) == e.x && (t.y ^ t.w) == e.y;
        float sl = ok ? (float)(int)(t.x - lag) : 0.f, st = ok ? (float)(int)(t.y - now) : 0.f;
        sl += dpp<X1>(sl); st += dpp<X1>(st); sl += dpp<X2>(sl); st += dpp<X2>(st); sl += dpp<X7>(sl); st += dpp<X7>(st);
        const int n = __builtin_popcountll(__builtin_amdgcn_ballot_w64(ok));
        if (n >= 4) {
          const float w = rcp((float)(n + 1));
          ref = lag + uni((unsigned)(int)(sl * w));
          t_ref = now + uni((unsigned)(int)(st * w));
          if (nref == 1) { ref_first = ref; t_first = t_ref; }
          else if (nref > 1) drift = (float)(int)(ref - ref_first) * rcp((float)(int)(t_ref - t_first) + 200.f);
          ++nref;
        }
      }
      if (lane == 0 && begun) *(volatile LAS v2u*)(L + L_REF) = v2u{nref ? ref + (unsigned)(int)(drift * (float)(int)(now - t_ref)) : lag, ~0u};
      __builtin_amdgcn_s_sleep(10);
    }
    return;
  }

  if (wave == 5) {
    __builtin_amdgcn_s_barrier();
    wait_word<4>(L + L_CNT);
    Pairs w[8];
#pragma unroll
    for (int j = 0; j < 8; ++j) {
      w[j] = pair_rows(*(const LAS v4u*)(L + L_WO + 2 * j * 1024 + lane * 16), *(const LAS v4u*)(L + L_WO + (2 * j + 1) * 1024 + lane * 16));
#pragma unroll
      for (int k = 0; k < 8; ++k) pin(w[j].c[k]);
    }
    int off[4], a_pa = WS_A + (g * 32 + s) * ROW + lane * 16;
#pragma unroll
    for (int i = 0; i < 4; ++i) { off[i] = WS_O + (gx * 32 + rho + 8 * i) * ROW + s * 128 + c * 16; pin(off[i]); }
    pin(a_pa);
    lds_barrier();
    const v2u e = *(const LAS v2u*)(L + L_E);
    __builtin_amdgcn_s_sleep(D1);
    v4u t[4];
    gather<4, true>(rs, off, e, t);
    const float m0 = dpp<0xA0>(asf(t[0].z)), m1 = dpp<0xA0>(asf(t[1].z)), m2 = dpp<0xA0>(asf(t[2].z)), m3 = dpp<0xA0>(asf(t[3].z));
    float M = fmaxf(fmaxf(m0, m1), fmaxf(m2, m3));
    M = rows_max(fmaxf(M, dpp<R8>(M)));
    const float f0 = ex2((m0 - M) * LOG2E), f1 = ex2((m1 - M) * LOG2E), f2_ = ex2((m2 - M) * LOG2E), f3 = ex2((m3 - M) * LOG2E);
    float o0 = fmaf(f0, asf(t[0].x), f1 * asf(t[1].x)) + fmaf(f2_, asf(t[2].x), f3 * asf(t[3].x));
    float o1 = fmaf(f0, asf(t[0].y), f1 * asf(t[1].y)) + fmaf(f2_, asf(t[2].y), f3 * asf(t[3].y));
    float ls = dpp<0xF5>(fmaf(f0, asf(t[0].z), f1 * asf(t[1].z)) + fmaf(f2_, asf(t[2].z), f3 * asf(t[3].z)));
    o0 += dpp<R8>(o0); o1 += dpp<R8>(o1); ls += dpp<R8>(ls);
    o0 = rows_add(o0); o1 = rows_add(o1); ls = rcp(rows_add(ls));
    const unsigned op = pk(o0 * ls, o1 * ls);
    unsigned oj[8];
#pragma unroll
    for (int j = 0; j < 8; ++j) oj[j] = __builtin_amdgcn_readlane(op, j);
    __builtin_amdgcn_sched_barrier(0);
    float acc[8] = {0.f, 0.f, 0.f, 0.f, 0.f, 0.f, 0.f, 0.f};
#pragma unroll
    for (int j = 0; j < 8; ++j) mac(w[j], oj[j], acc);
#pragma unroll
    for (int k = 0; k < 4; ++k) put2(rs, a_pa + k * 1024, asu(acc[2 * k]), asu(acc[2 * k + 1]), e);
    if (lane == 0) *(volatile LAS unsigned*)(L + L_FLAG) = 2u;
    return;
  }

  __builtin_amdgcn_s_barrier();
  if (s >= NFFN) return;
  const int sl = 4 * (g * 8 + (s & 7)) + (lane & 3), cp = 4 * (sl & 63) + (sl >> 6);
  const v4u xa = *(const GAS v4u*)(p.x + 8 * lane);
  const v4u wr = *(const GAS v4u*)(p.wr + 8 * lane);
  const unsigned xx = *(const GAS unsigned*)(p.x + 2 * cp);
  const v4u xb = *(const GAS v4u*)(p.x + g * 64 + 8 * rho);
  float mean, rden;
  ln_stats(xa, mean, rden);
  wait_word<4>(L + L_CNT);
  Pairs wd[4];
  unsigned gu0, gu1;
  {
    const v4u n = ln_apply(xb, mean, rden);
    float ga[8] = {0.f, 0.f, 0.f, 0.f, 0.f, 0.f, 0.f, 0.f}, ua[8] = {0.f, 0.f, 0.f, 0.f, 0.f, 0.f, 0.f, 0.f};
#pragma unroll
    for (int i = 0; i < 4; ++i) {
      const LAS unsigned char* w = L + 2 * i * 1024 + lane * 16;
      mac(pair_rows(*(const LAS v4u*)(w + L_WG), *(const LAS v4u*)(w + L_WG + 1024)), n[i], ga);
      mac(pair_rows(*(const LAS v4u*)(w + L_WU), *(const LAS v4u*)(w + L_WU + 1024)), n[i], ua);
      wd[i] = pair_rows(*(const LAS v4u*)(w + L_WD), *(const LAS v4u*)(w + L_WD + 1024));
#pragma unroll
      for (int k = 0; k < 8; ++k) pin(wd[i].c[k]);
    }
    const f2 gs = reduce8(ga), us = reduce8(ua);
    gu0 = asu((rho & 1) ? us.x : gs.x); gu1 = asu((rho & 1) ? us.y : gs.y);
  }
  const v4u nx = ln_apply(xa, mean, rden);
  const float r = rbf(wave_sum(dot2(nx.x, wr.x, dot2(nx.y, wr.y, 0.f)) + dot2(nx.z, wr.z, dot2(nx.w, wr.w, 0.f))));
  float sg = rbf(rcp(1.0f + ex2(-LOG2E * r)));
  const unsigned xnp = ln_pair(xx, mean, rden);
  float xn1 = lane < 32 ? lo(xnp) : hi(xnp), x1 = lane < 32 ? lo(xx) : hi(xx);
  int og[8], oy[2], a_py = WS_Y + s * ROW + (g * 4 + (lane >> 4)) * 128 + c * 16;
#pragma unroll
  for (int i = 0; i < 8; ++i) og[i] = WS_GU + (s * 8 + i) * 1024 + lane * 16;
  const bool two = (lane >> 2) + 16 < NFFN;
  oy[0] = WS_Y + ((cp >> 5) * 4 + (cp & 3)) * 128 + ((cp >> 2) & 7) * 16 + (lane >> 2) * ROW;
  oy[1] = oy[0] + (two ? 16 * ROW : 0);
  pin(oy[0]); pin(oy[1]);
  pin(gu0); pin(gu1); pin(sg); pin(xn1); pin(x1); pin(a_py);
  wait_word<1>(L + L_FLAG);
  const v2u e = *(const LAS v2u*)(L + L_E);
#pragma unroll 1
  for (int it = 0; it < 65536 && progress(L) < 4 * (NSTEP - RING); ++it) __builtin_amdgcn_s_sleep(4);
  put2(rs, WS_GU + (s * 8 + g) * 1024 + lane * 16, gu0, gu1, e);
  lds_barrier();
  v4u gu[8];
  gather<8, false>(rs, og, e, gu);
  const unsigned ap = pk(((asf(gu[0].x) + asf(gu[1].x)) + (asf(gu[2].x) + asf(gu[3].x))) + ((asf(gu[4].x) + asf(gu[5].x)) + (asf(gu[6].x) + asf(gu[7].x))),
                         ((asf(gu[0].y) + asf(gu[1].y)) + (asf(gu[2].y) + asf(gu[3].y))) + ((asf(gu[4].y) + asf(gu[5].y)) + (asf(gu[6].y) + asf(gu[7].y))));
  const float a0 = lo(ap), a1 = hi(ap);
  const unsigned sp = pk(a0 * rcp(1.0f + ex2(-LOG2E * a0)), a1 * rcp(1.0f + ex2(-LOG2E * a1)));
  const int hp = (int)pk(lo(sp) * dpp<R8>(a0), hi(sp) * dpp<R8>(a1));
  float y[8] = {0.f, 0.f, 0.f, 0.f, 0.f, 0.f, 0.f, 0.f};
#pragma unroll
  for (int i = 0; i < 4; ++i) mac(wd[i], (unsigned)__builtin_amdgcn_ds_bpermute((16 * i + rho) * 4, hp), y);
  const f2 ys = reduce8(y);
  if ((rho & 1) == 0) put2(rs, a_py, asu(ys.x), asu(ys.y), e);
  if (s >= 8) return;
  __builtin_amdgcn_s_sleep(DY);
  v4u yv[2];
  gather<2, false>(rs, oy, e, yv);
  const float yc = rbf(reduce_quads(asf(yv[0].x) + (two ? asf(yv[1].x) : 0.f), asf(yv[0].y) + (two ? asf(yv[1].y) : 0.f)));
  if ((lane & 28) == 0) *(LAS f2*)(L + L_B + ((lane >> 5) * 4 + (lane & 3)) * 8) = f2{rbf(xn1 + rbf(yc * sg)), x1};
  asm volatile("s_waitcnt lgkmcnt(0)" ::: "memory");
}

extern "C" __device__ __attribute__((used, noinline)) void fused_decode_code_end() {}

extern "C" void kernel_launch(void* const* d_in, const int* in_sizes, int n_in, void* d_out, int out_size, void* d_ws, size_t ws_size,
                              hipStream_t stream) {
  (void)in_sizes; (void)n_in; (void)out_size;
  Params p;
  p.x = (const unsigned short*)d_in[0];
  p.k = (const unsigned short*)d_in[1];
  p.v = (const unsigned short*)d_in[2];
  p.wq = (const unsigned short*)d_in[3];
  p.wo = (const unsigned short*)d_in[4];
  p.wr = (const unsigned short*)d_in[6];
  p.wg = (const unsigned short*)d_in[7];
  p.wu = (const unsigned short*)d_in[8];
  p.wd = (const unsigned short*)d_in[9];
  p.out = (unsigned*)d_out;
  p.ws = (unsigned char*)d_ws + (ws_size - WS_SPAN) / 4096 * 4096;
  hipLaunchKernelGGL(fused_decode_kernel, dim3(NWG), dim3(NTHR), 0, stream, p);
}
```

```cpp
#include <hip/hip_runtime.h>
#include <hip/hip_bf16.h>
#include <stdint.h>

#define GAS __attribute__((address_space(1)))
#define LAS __attribute__((address_space(3)))
typedef unsigned v4u __attribute__((ext_vector_type(4)));
typedef unsigned v2u __attribute__((ext_vector_type(2)));
typedef float f4 __attribute__((ext_vector_type(4)));
typedef float f2 __attribute__((ext_vector_type(2)));
typedef short s16x4 __attribute__((ext_vector_type(4)));
typedef __bf16 bf16x8 __attribute__((ext_vector_type(8)));
typedef __bf16 bf16x2 __attribute__((ext_vector_type(2)));

#define END_MARK fused_decode_code_end
#define KERNEL_NAME fused_decode_kernel
#define STR2(x) #x
#define STR(x) STR2(x)
#define NSTEP_RUN NSTEP
#define HOLDT 0
#define WSTAIL 1
#define ADAPT 1
#define PACE0 4
#define KP 6
#define UNOM 576
#define NEXCH 8
#define XSTOP 208
#define PUBM 3
#define CSLEEP 10
#define TAU16 218
#define D1 18
#define D2 10
#define DY 10
#define T1 0
#define T2 0
#define GPROG 240

namespace {

constexpr int D = 512, DH = 128, SK = 65536, NQ = 4096, FF = 1792;
constexpr int NWG = 256, NTHR = 448;
constexpr int KEYS_PER_WG = SK / 32;
constexpr int NSTEP = KEYS_PER_WG / 32;
constexpr int RING = 4;
constexpr int NFFN = FF / 64;
constexpr float SCALE = 0.08838834764831845f, LOG2E = 1.4426950408889634f, FMIN = -3.4028234664e38f;
constexpr unsigned SPIN_CAP = 1u << 16;

constexpr int ROW = 4096 + 128;
constexpr int WS_EPOCH = 0;
constexpr int WS_Q = 4096;
constexpr int WS_GU = WS_Q + 8 * 4 * 4096;
constexpr int WS_Y = WS_GU + NFFN * 8192;
constexpr int WS_O = WS_Y + NFFN * ROW;
constexpr int WS_A = WS_O + 256 * ROW;
constexpr int WS_PL = WS_A + 256 * ROW;
constexpr int WS_END = WS_PL + 256 * 128;
constexpr size_t WS_SPAN = 4u << 20;
static_assert(WS_END + 65536 <= WS_SPAN, "workspace map");

constexpr int L_Q = 0, L_E = 1024, L_FLAG = 1032, L_CNT = 1036, L_PROG = 1040, L_LVL = 1056, L_TEND = 1060, L_B = 1072, L_FIN = 1136;
constexpr int L_ML = 1280;
constexpr int REC = 4 * 132 * 4, L_REC = 1536;
constexpr int L_WO = 16384, L_WG = 32768, L_WU = 40960, L_WD = 49152;
constexpr int LDS_BYTES = 84 * 1024;

struct Params {
  const unsigned short *x, *k, *v, *wq, *wo, *wr, *wg, *wu, *wd;
  unsigned* out;
  unsigned char* ws;
};

__device__ __forceinline__ float asf(unsigned u) { return __builtin_bit_cast(float, u); }
__device__ __forceinline__ unsigned asu(float f) { return __builtin_bit_cast(unsigned, f); }
__device__ __forceinline__ float lo(unsigned u) { return asf(u << 16); }
__device__ __forceinline__ float hi(unsigned u) { return asf(u & 0xffff0000u); }
__device__ __forceinline__ unsigned pk(float a, float b) {
  bf16x2 r = {(__bf16)a, (__bf16)b};
  return __builtin_bit_cast(unsigned, r);
}
__device__ __forceinline__ float rbf(float a) { return lo(pk(a, a)); }
__device__ __forceinline__ float rcp(float a) { return __builtin_amdgcn_rcpf(a); }
__device__ __forceinline__ float ex2(float a) { return __builtin_amdgcn_exp2f(a); }
__device__ __forceinline__ unsigned xor3(unsigned a, unsigned b, unsigned c) { return __builtin_amdgcn_bitop3_b32(a, b, c, 0x96); }
__device__ __forceinline__ unsigned or3(unsigned a, unsigned b, unsigned c) { return __builtin_amdgcn_bitop3_b32(a, b, c, 0xFE); }
__device__ __forceinline__ unsigned uni(unsigned a) { return (unsigned)__builtin_amdgcn_readfirstlane((int)a); }
__device__ __forceinline__ unsigned clock32() { return (unsigned)__builtin_amdgcn_s_memrealtime(); }
__device__ __forceinline__ float dot2(unsigned a, unsigned b, float c) {
  return __builtin_amdgcn_fdot2_f32_bf16(__builtin_bit_cast(bf16x2, a), __builtin_bit_cast(bf16x2, b), c, false);
}
template <class T> __device__ __forceinline__ void pin(T& x) { asm volatile("" : "+v"(x)); }
template <int CTRL> __device__ __forceinline__ float dpp(float x) {
  return asf(__builtin_amdgcn_mov_dpp(__builtin_bit_cast(int, x), CTRL, 0xf, 0xf, true));
}
constexpr int X1 = 0xB1, X2 = 0x4E, X7 = 0x141, R4 = 0x124, R8 = 0x128;
__device__ __forceinline__ float x32_tr(float a, float b) {
  auto s = __builtin_amdgcn_permlane32_swap(asu(a), asu(b), false, false);
  return asf(s[0]) + asf(s[1]);
}
__device__ __forceinline__ float x16_tr(float a, float b) {
  auto s = __builtin_amdgcn_permlane16_swap(asu(a), asu(b), false, false);
  return asf(s[0]) + asf(s[1]);
}
__device__ __forceinline__ float rows_add(float x) { x = x16_tr(x, x); return x32_tr(x, x); }
__device__ __forceinline__ float rows_max(float x) {
  auto s = __builtin_amdgcn_permlane16_swap(asu(x), asu(x), false, false);
  x = fmaxf(asf(s[0]), asf(s[1]));
  auto t = __builtin_amdgcn_permlane32_swap(asu(x), asu(x), false, false);
  return fmaxf(asf(t[0]), asf(t[1]));
}
__device__ __forceinline__ float wave_sum(float x) {
  x += dpp<X1>(x); x += dpp<X2>(x); x += dpp<X7>(x); x += dpp<R8>(x);
  return rows_add(x);
}
__device__ __forceinline__ void lds_barrier() {
  asm volatile("s_waitcnt lgkmcnt(0)" ::: "memory");
  __builtin_amdgcn_s_barrier();
  asm volatile("" ::: "memory");
}
template <int N> __device__ __forceinline__ void pace() {
  __builtin_amdgcn_sched_barrier(0);
  asm volatile("s_waitcnt vmcnt(%0)" :: "i"(N) : "memory");
  __builtin_amdgcn_sched_barrier(0);
}
__device__ __forceinline__ void pace_lvl(unsigned lvl) {
  __builtin_amdgcn_sched_barrier(0);
  asm volatile("s_cmp_gt_u32 %0, 3\n\ts_cbranch_scc1 4f\n\ts_cmp_gt_u32 %0, 1\n\ts_cbranch_scc1 2f\n\t"
               "s_cmp_eq_u32 %0, 1\n\ts_cbranch_scc1 1f\n\ts_waitcnt vmcnt(2)\n1:\n\ts_waitcnt vmcnt(3)\n\ts_branch 9f\n"
               "2:\n\ts_cmp_eq_u32 %0, 3\n\ts_cbranch_scc1 3f\n\ts_waitcnt vmcnt(4)\n3:\n\ts_waitcnt vmcnt(5)\n\ts_branch 9f\n"
               "4:\n\ts_cmp_gt_u32 %0, 5\n\ts_cbranch_scc1 6f\n\t"
               "s_cmp_eq_u32 %0, 5\n\ts_cbranch_scc1 5f\n\ts_waitcnt vmcnt(6)\n5:\n\ts_waitcnt vmcnt(7)\n\ts_branch 9f\n"
               "6:\n\ts_cmp_gt_u32 %0, 7\n\ts_cbranch_scc1 9f\n\t"
               "s_cmp_eq_u32 %0, 7\n\ts_cbranch_scc1 7f\n\ts_waitcnt vmcnt(8)\n7:\n\ts_waitcnt vmcnt(10)\n9:"
               :: "s"(lvl) : "scc");
  __builtin_amdgcn_sched_barrier(0);
}
constexpr int SC1 = 16, NT = 2;
__device__ __forceinline__ void put2(__amdgpu_buffer_rsrc_t rs, int off, unsigned a, unsigned b, v2u e) {
  __builtin_amdgcn_raw_buffer_store_b128(v4u{a, b, a ^ e.x, b ^ e.y}, rs, off, 0, SC1);
}
__device__ __forceinline__ void put3(__amdgpu_buffer_rsrc_t rs, int off, unsigned a, unsigned b, unsigned c, v2u e) {
  __builtin_amdgcn_raw_buffer_store_b128(v4u{a, b, c, xor3(a, b, c) ^ e.x}, rs, off, 0, SC1);
}
template <int N, bool THREE> __device__ __forceinline__ void gather(__amdgpu_buffer_rsrc_t rs, const int (&off)[N], v2u e, v4u (&g)[N]) {
#pragma unroll 1
  for (unsigned spin = 0; spin < SPIN_CAP; ++spin) {
    asm volatile("" ::: "memory");
#pragma unroll
    for (int i = 0; i < N; ++i) g[i] = __builtin_amdgcn_raw_buffer_load_b128(rs, off[i], 0, SC1);
    unsigned bad = 0;
#pragma unroll
    for (int i = 0; i < N; ++i) {
      if (THREE) bad |= xor3(xor3(g[i].x, g[i].y, g[i].z), g[i].w, e.x);
      else bad = or3(xor3(g[i].x, g[i].z, e.x), xor3(g[i].y, g[i].w, e.y), bad);
    }
    if (__builtin_amdgcn_ballot_w64(bad != 0u) == 0ull) break;
  }
}
template <unsigned V> __device__ __forceinline__ void wait_word(LAS unsigned char* w) {
#pragma unroll 1
  for (unsigned spin = 0; spin < (1u << 22); ++spin) {
    if (uni(*(volatile LAS unsigned*)w) >= V) break;
    __builtin_amdgcn_s_sleep(1);
  }
  asm volatile("" ::: "memory");
}
__device__ __forceinline__ unsigned progress(LAS unsigned char* L) {
  const v4u pr = *(volatile LAS v4u*)(L + L_PROG);
  return uni(pr.x + pr.y + pr.z + pr.w);
}
__device__ __forceinline__ void wait_until(unsigned t) {
#pragma unroll 1
  for (int i = 0; i < 128 && (int)(clock32() - t) < 0; ++i) {}
}
__device__ __forceinline__ void dma(const void* src, unsigned dst) {
  unsigned keep;
  asm volatile("s_mov_b32 %0, m0\n\ts_mov_b32 m0, %2\n\ts_nop 0\n\tglobal_load_lds_dwordx4 %1, off nt\n\ts_mov_b32 m0, %0"
               : "=&s"(keep) : "v"(src), "s"(dst) : "memory");
}
__device__ __forceinline__ void ln_stats(v4u xa, float& mean, float& rden) {
  const unsigned one = 0x3f803f80u;
  const float mu = wave_sum(dot2(xa.x, one, dot2(xa.y, one, 0.f)) + dot2(xa.z, one, dot2(xa.w, one, 0.f))) * (1.0f / D);
  float q0 = 0.f, q1 = 0.f;
#pragma unroll
  for (int j = 0; j < 4; ++j) { const float a = lo(xa[j]) - mu, b = hi(xa[j]) - mu; q0 = fmaf(a, a, q0); q1 = fmaf(b, b, q1); }
  const float var = rbf(wave_sum(q0 + q1) * (1.0f / D));
  mean = rbf(mu);
  rden = rcp(rbf(__builtin_amdgcn_sqrtf(rbf(var + 1e-5f))));
}
__device__ __forceinline__ unsigned ln_pair(unsigned xx, float mean, float rden) {
  const unsigned d = pk(lo(xx) - mean, hi(xx) - mean);
  return pk(lo(d) * rden, hi(d) * rden);
}
__device__ __forceinline__ v4u ln_apply(v4u xa, float mean, float rden) {
  return v4u{ln_pair(xa.x, mean, rden), ln_pair(xa.y, mean, rden), ln_pair(xa.z, mean, rden), ln_pair(xa.w, mean, rden)};
}
struct Pairs { unsigned c[8]; };
__device__ __forceinline__ Pairs pair_rows(v4u w0, v4u w1) {
  Pairs r;
#pragma unroll
  for (int k = 0; k < 4; ++k) {
    r.c[2 * k] = __builtin_amdgcn_perm(w1[k], w0[k], 0x05040100u);
    r.c[2 * k + 1] = __builtin_amdgcn_perm(w1[k], w0[k], 0x07060302u);
  }
  return r;
}
__device__ __forceinline__ void mac(const Pairs& w, unsigned x, float (&acc)[8]) {
#pragma unroll
  for (int k = 0; k < 8; ++k) acc[k] = dot2(w.c[k], x, acc[k]);
}
__device__ __forceinline__ f2 reduce8(const float (&acc)[8]) {
  const float t0 = x32_tr(acc[0], acc[4]), t1 = x32_tr(acc[1], acc[5]), t2 = x32_tr(acc[2], acc[6]), t3 = x32_tr(acc[3], acc[7]);
  const float u0 = x16_tr(t0, t2), u1 = x16_tr(t1, t3);
  return f2{u0 + dpp<R8>(u0), u1 + dpp<R8>(u1)};
}
__device__ __forceinline__ float reduce_quads(float a, float b) {
  float t = x32_tr(a, b);
  t = x16_tr(t, t);
  t += dpp<R4>(t);
  return t + dpp<R8>(t);
}
#define STAMP(i) do {} while (0)
#define FLUSH(a, b) do {} while (0)

struct Acc { f4 o0, o1; float m, l; };

__device__ __forceinline__ unsigned scores_to_p(v4u ka, v4u kb, bf16x8 qf, Acc& s) {
  const f4 z = {0.f, 0.f, 0.f, 0.f};
  const f4 da = __builtin_amdgcn_mfma_f32_16x16x32_bf16(__builtin_bit_cast(bf16x8, ka), qf, z, 0, 0, 0);
  const f4 db = __builtin_amdgcn_mfma_f32_16x16x32_bf16(__builtin_bit_cast(bf16x8, kb), qf, z, 0, 0, 0);
  float sa = (dpp<0x00>(da[0]) + dpp<0x55>(da[1])) + (dpp<0xAA>(da[2]) + dpp<0xFF>(da[3]));
  float sb = (dpp<0x00>(db[0]) + dpp<0x55>(db[1])) + (dpp<0xAA>(db[2]) + dpp<0xFF>(db[3]));
  unsigned u = pk(sa, sb);
  u = pk(lo(u) * SCALE, hi(u) * SCALE);
  sa = lo(u); sb = hi(u);
  float mx = fmaxf(sa, sb);
  if (__builtin_amdgcn_ballot_w64(mx > s.m) != 0ull) {
    mx = fmaxf(s.m, rows_max(mx));
    const float al = ex2((s.m - mx) * LOG2E);
    s.o0 *= al; s.o1 *= al; s.l *= al; s.m = mx;
  }
  const float pa = ex2((sa - s.m) * LOG2E), pb = ex2((sb - s.m) * LOG2E);
  s.l += pa + pb;
  return pk(pa, pb);
}
__device__ __forceinline__ void pv(v4u va, v4u vb, unsigned pp, v4u sel, Acc& s) {
  const v2u ba = {__builtin_amdgcn_perm(pp, pp, sel.x), __builtin_amdgcn_perm(pp, pp, sel.y)};
  const v2u bb = {__builtin_amdgcn_perm(pp, pp, sel.z), __builtin_amdgcn_perm(pp, pp, sel.w)};
  s.o0 = __builtin_amdgcn_mfma_f32_16x16x16bf16_1k(__builtin_bit_cast(s16x4, v2u{va.x, va.y}), __builtin_bit_cast(s16x4, ba), s.o0, 0, 0, 0);
  s.o1 = __builtin_amdgcn_mfma_f32_16x16x16bf16_1k(__builtin_bit_cast(s16x4, v2u{va.z, va.w}), __builtin_bit_cast(s16x4, ba), s.o1, 0, 0, 0);
  s.o0 = __builtin_amdgcn_mfma_f32_16x16x16bf16_1k(__builtin_bit_cast(s16x4, v2u{vb.x, vb.y}), __builtin_bit_cast(s16x4, bb), s.o0, 0, 0, 0);
  s.o1 = __builtin_amdgcn_mfma_f32_16x16x16bf16_1k(__builtin_bit_cast(s16x4, v2u{vb.z, vb.w}), __builtin_bit_cast(s16x4, bb), s.o1, 0, 0, 0);
}

}

extern "C" __global__ void __launch_bounds__(NTHR, 2) KERNEL_NAME(Params p) {
  __shared__ __attribute__((aligned(16))) unsigned char smem[LDS_BYTES];
  LAS unsigned char* L = (LAS unsigned char*)smem;
  unsigned long long code;
  int code_len;
  asm volatile("s_getpc_b64 %0\n\ts_mov_b32 %1, " STR(END_MARK) "@rel32@lo+4" : "=s"(code), "=s"(code_len));
  const int lane = threadIdx.x & 63, wave = __builtin_amdgcn_readfirstlane(threadIdx.x >> 6);
  const int b = blockIdx.x, g = b & 7, s = b >> 3;
  const int gx = g ^ 1;
  const __amdgpu_buffer_rsrc_t rs = __builtin_amdgcn_make_buffer_rsrc((void*)p.ws, 0, WS_END, 0x00020000);

  if (wave < 4) {
    const size_t base = ((size_t)g * SK + (size_t)s * KEYS_PER_WG) * DH;
    const __amdgpu_buffer_rsrc_t rk = __builtin_amdgcn_make_buffer_rsrc((void*)(p.k + base), 0, KEYS_PER_WG * DH * 2, 0x00020000);
    const __amdgpu_buffer_rsrc_t rv = __builtin_amdgcn_make_buffer_rsrc((void*)(p.v + base), 0, KEYS_PER_WG * DH * 2, 0x00020000);
    const int h = (lane >> 2) & 3, c = lane & 3;
    const int frag = h * 256 + (lane >> 4) * 64 + c * 16;
    int ko = wave * 2048 + frag, vo = wave * 2048 + lane * 16;
    constexpr int STRIDE = 32 * DH * 2;
    if (threadIdx.x == 0) {
      *(volatile LAS v2u*)(L + L_FLAG) = v2u{0u, 0u};
      *(volatile LAS v4u*)(L + L_PROG) = v4u{0u, 0u, 0u, 0u};
      *(volatile LAS v2u*)(L + L_LVL) = v2u{2u, 0u};
    }
    lds_barrier();
    {
      const unsigned lb = (unsigned)(size_t)L;
      const unsigned short* wo = p.wo + (size_t)(gx * 512 + s * 16 + 4 * wave) * D + 8 * lane;
#pragma unroll
      for (int i = 0; i < 4; ++i) dma(wo + i * D, lb + L_WO + (4 * wave + i) * 1024);
      if (s < NFFN) {
        const int row = 8 * (lane >> 3) + 2 * wave;
        const size_t gu = (size_t)(g * 64 + row) * FF + s * 64 + (lane & 7) * 8;
        const unsigned short* wd = p.wd + (size_t)(s * 64 + row) * D + g * 64 + (lane & 7) * 8;
        dma(p.wg + gu, lb + L_WG + 2 * wave * 1024);
        dma(p.wu + gu, lb + L_WU + 2 * wave * 1024);
        pace<PACE0>();
        dma(p.wg + gu + FF, lb + L_WG + (2 * wave + 1) * 1024);
        dma(p.wu + gu + FF, lb + L_WU + (2 * wave + 1) * 1024);
        pace<PACE0>();
        dma(wd, lb + L_WD + 2 * wave * 1024);
        dma(wd + D, lb + L_WD + (2 * wave + 1) * 1024);
      }
    }
    v4u ka[RING], kb[RING], va[RING], vb[RING];
#pragma unroll
    for (int r = 0; r < RING; ++r) {
      pace<PACE0>();
      if (r == (PACE0 + 3) / 4 && lane == 0)
        __hip_atomic_fetch_add((LAS unsigned*)(L + L_CNT), 1u, __ATOMIC_RELAXED, __HIP_MEMORY_SCOPE_WORKGROUP);
      ka[r] = __builtin_amdgcn_raw_buffer_load_b128(rk, ko, 0, NT);
      kb[r] = __builtin_amdgcn_raw_buffer_load_b128(rk, ko + 1024, 0, NT);
      pace<PACE0>();
      va[r] = __builtin_amdgcn_raw_buffer_load_b128(rv, vo, 0, NT);
      vb[r] = __builtin_amdgcn_raw_buffer_load_b128(rv, vo + 1024, 0, NT);
      ko += STRIDE; vo += STRIDE;
    }
    __builtin_amdgcn_sched_barrier(0);
    const unsigned z = 0x0c0c0c0cu;
    const v4u sel = {c == 0 ? 0x0c0c0100u : c == 1 ? 0x01000c0cu : z, c == 2 ? 0x0c0c0100u : c == 3 ? 0x01000c0cu : z,
                     c == 0 ? 0x0c0c0302u : c == 1 ? 0x03020c0cu : z, c == 2 ? 0x0c0c0302u : c == 3 ? 0x03020c0cu : z};
    const int id = s * 8 + g;
    int a_ml = L_ML + h * 32 + wave * 8, a_ow = L_REC + wave * REC + (h * 132 + (lane >> 4) * 32 + c) * 4, a_or = L_REC + (wave * 132 + 2 * lane) * 4;
    int a_po = WS_O + (g * 32 + s) * ROW + threadIdx.x * 16;
    pin(a_ml); pin(a_ow); pin(a_or); pin(a_po);
    STAMP(14);
    wait_word<1>(L + L_FLAG);
    const bf16x8 qf = __builtin_bit_cast(bf16x8, *(const LAS v4u*)(L + L_Q + frag));
    v2u e = *(const LAS v2u*)(L + L_E);
    pin(e);
    STAMP(1);
    Acc a;
    a.o0 = f4{0.f, 0.f, 0.f, 0.f}; a.o1 = a.o0; a.m = FMIN; a.l = 0.f;
#pragma unroll 1
    for (int i = NSTEP_RUN - RING; i >= 0; i -= RING) {
#pragma unroll
      for (int r = 0; r < RING; ++r) {
        unsigned lv = *(volatile LAS unsigned*)(L + L_LVL);
        *(volatile LAS unsigned*)(L + L_PROG + 4 * wave) = (unsigned)(NSTEP - RING + r - i);
        const unsigned pp = scores_to_p(ka[r], kb[r], qf, a);
        lv = i ? uni(lv) : 8u;
        pace_lvl(lv);
        ka[r] = __builtin_amdgcn_raw_buffer_load_b128(rk, ko, 0, NT);
        kb[r] = __builtin_amdgcn_raw_buffer_load_b128(rk, ko + 1024, 0, NT);
        if (r == RING - 1) *(volatile LAS f2*)(L + a_ml) = f2{a.m, rows_add(a.l)};
        __builtin_amdgcn_sched_barrier(0);
        pv(va[r], vb[r], pp, sel, a);
        pace_lvl(lv);
        va[r] = __builtin_amdgcn_raw_buffer_load_b128(rv, vo, 0, NT);
        vb[r] = __builtin_amdgcn_raw_buffer_load_b128(rv, vo + 1024, 0, NT);
        ko += STRIDE; vo += STRIDE;
        __builtin_amdgcn_sched_barrier(0);
      }
    }
    STAMP(2);
#pragma unroll
    for (int i = 0; i < 4; ++i) { *(LAS float*)(L + a_ow + 32 * i) = a.o0[i]; *(LAS float*)(L + a_ow + 32 * i + 16) = a.o1[i]; }
    lds_barrier();
    {
      const f4 ml0 = *(const LAS f4*)(L + L_ML + wave * 32), ml1 = *(const LAS f4*)(L + L_ML + wave * 32 + 16);
      const f2 t0 = *(const LAS f2*)(L + a_or), t1 = *(const LAS f2*)(L + a_or + REC), t2 = *(const LAS f2*)(L + a_or + 2 * REC), t3 = *(const LAS f2*)(L + a_or + 3 * REC);
      __builtin_amdgcn_sched_barrier(0);
      const float M = fmaxf(fmaxf(ml0.x, ml0.z), fmaxf(ml1.x, ml1.z));
      const float f0 = ex2((ml0.x - M) * LOG2E), f1 = ex2((ml0.z - M) * LOG2E), f2_ = ex2((ml1.x - M) * LOG2E), f3 = ex2((ml1.z - M) * LOG2E);
      const f2 o = (f0 * t0 + f1 * t1) + (f2_ * t2 + f3 * t3);
      const float ls = fmaf(f0, ml0.y, f1 * ml0.w) + fmaf(f2_, ml1.y, f3 * ml1.w);
      put3(rs, a_po, asu(o.x), asu(o.y), asu((lane & 1) ? ls : M), e);
    }
    STAMP(3);
    if (wave == 0) { FLUSH(0, 3); FLUSH(14, 14); }
    if (s >= 8) return;
    int off[4];
#pragma unroll
    for (int i = 0; i < 4; ++i) { off[i] = WS_A + (64 * wave + 16 * i + (lane >> 2)) * ROW + (4 * id + c) * 16; pin(off[i]); }
    GAS unsigned short* po = (GAS unsigned short*)p.out + 2 * (16 * (id & 15) + 4 * c + (id >> 4)) + ((lane >> 2) & 1);
    int a_fw = L_FIN + wave * 32 + (lane >> 5) * 16 + c * 4;
    pin(po); pin(a_fw);
    const unsigned t_end = uni(*(volatile LAS unsigned*)(L + L_TEND));
    wait_word<2>(L + L_FLAG);
    if (D2) __builtin_amdgcn_s_sleep(D2);
    if (T2) wait_until(t_end + T2);
    v4u t[4];
    gather<4, false>(rs, off, e, t);
    STAMP(10);
    const float av = reduce_quads((asf(t[0].x) + asf(t[1].x)) + (asf(t[2].x) + asf(t[3].x)), (asf(t[0].y) + asf(t[1].y)) + (asf(t[2].y) + asf(t[3].y)));
    if ((lane & 28) == 0) *(LAS float*)(L + a_fw) = av;
    lds_barrier();
    if (wave == 0 && lane < 8) {
      const LAS float* fin = (const LAS float*)(L + L_FIN) + lane;
      const f2 bx = *(const LAS f2*)(L + L_B + lane * 8);
      const float r = rbf((fin[0] + fin[8]) + (fin[16] + fin[24]));
      *po = (unsigned short)pk(rbf(bx.y + r) + bx.x, 0.f);
      if (b == 0 && lane == 0) __builtin_amdgcn_raw_buffer_store_b64(e, rs, WS_EPOCH, 0, SC1);
    }
    STAMP(11);
    if (wave == 0) FLUSH(10, 11);
    return;
  }

  const int rho = lane >> 3, c = lane & 7;
  if (wave == 4) {
    const int kq = s & 3, strip = s >> 2;
    const int r0 = kq * 128 + 16 * rho;
    const v4u xa = *(const GAS v4u*)(p.x + 8 * lane);
    const v4u xb0 = *(const GAS v4u*)(p.x + r0), xb1 = *(const GAS v4u*)(p.x + r0 + 8);
    const v2u eo = __builtin_amdgcn_raw_buffer_load_b64(rs, WS_EPOCH, 0, SC1);
    const unsigned short* wp = p.wq + (size_t)r0 * NQ + gx * 512 + strip * 64 + c * 8;
    v4u w[16];
#pragma unroll
    for (int i = 0; i < 16; ++i) w[i] = __builtin_nontemporal_load((const GAS v4u*)(wp + (size_t)i * NQ));
    __builtin_amdgcn_sched_barrier(0);
    __builtin_amdgcn_s_barrier();
    STAMP(12);
    float mean, rden;
    ln_stats(xa, mean, rden);
    const v4u n0 = ln_apply(xb0, mean, rden), n1 = ln_apply(xb1, mean, rden);
    v2u e = {uni(eo.x) + 1u, uni(eo.y) + 1u};
    if (e.x == 0u) e.x = 1u;
    if (e.y == 0u) e.y = 1u;
    float acc[8] = {0.f, 0.f, 0.f, 0.f, 0.f, 0.f, 0.f, 0.f};
#pragma unroll
    for (int i = 0; i < 4; ++i) mac(pair_rows(w[2 * i], w[2 * i + 1]), n0[i], acc);
#pragma unroll
    for (int i = 0; i < 4; ++i) mac(pair_rows(w[8 + 2 * i], w[9 + 2 * i]), n1[i], acc);
    const f2 u = reduce8(acc);
    if ((rho & 1) == 0) put2(rs, WS_Q + (gx * 4 + kq) * 4096 + (strip * 32 + (lane >> 4) * 8 + c) * 16, asu(u.x), asu(u.y), e);
    STAMP(4);
    int off[16];
#pragma unroll
    for (int i = 0; i < 16; ++i) off[i] = WS_Q + (g * 4 + (i & 3)) * 4096 + ((i >> 2) * 64 + lane) * 16;
    v4u q[16];
    gather<16, false>(rs, off, e, q);
#pragma unroll
    for (int i = 0; i < 4; ++i)
      *(LAS unsigned*)(L + L_Q + (i * 64 + (lane & 32) + 4 * c + (rho & 3)) * 4) =
          pk((asf(q[4 * i].x) + asf(q[4 * i + 1].x)) + (asf(q[4 * i + 2].x) + asf(q[4 * i + 3].x)),
             (asf(q[4 * i].y) + asf(q[4 * i + 1].y)) + (asf(q[4 * i + 2].y) + asf(q[4 * i + 3].y)));
    if (lane == 0) *(LAS v2u*)(L + L_E) = e;
    asm volatile("s_waitcnt lgkmcnt(0)" ::: "memory");
    if (lane == 0) *(volatile LAS unsigned*)(L + L_FLAG) = 1u;
    STAMP(6);
    FLUSH(4, 4); FLUSH(6, 6); FLUSH(12, 12);
    if (ADAPT) {
      const bool rd = lane < 8 && lane != g;
      unsigned last = 0, t_ref = 0, ref = 0, t_first = 0, ref_first = 0;
      float drift = 0.f;
      int nref = 0;
#pragma unroll 1
      for (int it = 0; it < 8192; ++it) {
        const bool exch = it % NEXCH == 0 && last < XSTOP;
        v4u t = {0u, 0u, 0u, 0u};
        asm volatile("" ::: "memory");
        if (exch && it && rd) t = __builtin_amdgcn_raw_buffer_load_b128(rs, WS_PL + (((s + 5 * (it / NEXCH) + 3 * lane) & (31 & ~PUBM)) * 8 + lane) * 128, 0, SC1);
        asm volatile("" : "+v"(t) :: "memory");
        last = progress(L);
        const unsigned now = clock32(), lag = now - (last * TAU16 >> 4);
        if (exch) {
          if (lane == 0 && (s & PUBM) == 0) put2(rs, WS_PL + b * 128, lag, now, e);
          const bool ok = rd && (t.x ^ t.z) == e.x && (t.y ^ t.w) == e.y;
          float sl = ok ? (float)(int)(t.x - lag) : 0.f, st = ok ? (float)(int)(t.y - now) : 0.f;
          sl += dpp<X1>(sl); st += dpp<X1>(st); sl += dpp<X2>(sl); st += dpp<X2>(st); sl += dpp<X7>(sl); st += dpp<X7>(st);
          const int n = __builtin_popcountll(__builtin_amdgcn_ballot_w64(ok));
          if (n >= 4) {
            const float w = rcp((float)(n + 1));
            ref = lag + uni((unsigned)(int)(sl * w));
            t_ref = now + uni((unsigned)(int)(st * w));
            if (nref == 1) { ref_first = ref; t_first = t_ref; }
            else if (nref > 1) drift = (float)(int)(ref - ref_first) * rcp((float)(int)(t_ref - t_first) + 200.f);
            ++nref;
          }
        }
        const int lead = nref ? (int)(ref - lag + uni((unsigned)(int)(drift * (float)(int)(now - t_ref)))) : 0;
        if (last >= 4 * (NSTEP - RING)) {
          if (lane == 0) *(volatile LAS unsigned*)(L + L_TEND) = lag + (unsigned)lead + (4 * (NSTEP - RING) * TAU16 >> 4);
          break;
        }
        if (lane == 0) *(volatile LAS unsigned*)(L + L_LVL) = (unsigned)min(max((UNOM - lead * KP) >> 8, 0), 7);
        __builtin_amdgcn_s_sleep(CSLEEP);
      }
      asm volatile("s_waitcnt lgkmcnt(0)" ::: "memory");
    }
    return;
  }

  if (wave == 5) {
    const v4u cpf = __builtin_amdgcn_raw_buffer_load_b128(
        __builtin_amdgcn_make_buffer_rsrc((void*)code, 0, code_len > 0 && code_len <= 32768 ? code_len & ~15 : 0, 0x00020000), s * 1024 + lane * 16, 0, 0);
    __builtin_amdgcn_sched_barrier(0);
    __builtin_amdgcn_s_barrier();
    asm volatile("" :: "v"(cpf));
    wait_word<4>(L + L_CNT);
    Pairs w[8];
#pragma unroll
    for (int j = 0; j < 8; ++j) {
      w[j] = pair_rows(*(const LAS v4u*)(L + L_WO + 2 * j * 1024 + lane * 16), *(const LAS v4u*)(L + L_WO + (2 * j + 1) * 1024 + lane * 16));
#pragma unroll
      for (int k = 0; k < 8; ++k) pin(w[j].c[k]);
    }
    int off[4], a_pa = WS_A + (g * 32 + s) * ROW + lane * 16;
#pragma unroll
    for (int i = 0; i < 4; ++i) { off[i] = WS_O + (gx * 32 + rho + 8 * i) * ROW + s * 128 + c * 16; pin(off[i]); }
    pin(a_pa);
    lds_barrier();
    const v2u e = *(const LAS v2u*)(L + L_E);
    const unsigned t_end = uni(*(volatile LAS unsigned*)(L + L_TEND));
    STAMP(16);
    if (D1) __builtin_amdgcn_s_sleep(D1);
    if (T1) wait_until(t_end + T1);
    v4u t[4];
    gather<4, true>(rs, off, e, t);
    STAMP(8);
    const float m0 = dpp<0xA0>(asf(t[0].z)), m1 = dpp<0xA0>(asf(t[1].z)), m2 = dpp<0xA0>(asf(t[2].z)), m3 = dpp<0xA0>(asf(t[3].z));
    float M = fmaxf(fmaxf(m0, m1), fmaxf(m2, m3));
    M = rows_max(fmaxf(M, dpp<R8>(M)));
    const float f0 = ex2((m0 - M) * LOG2E), f1 = ex2((m1 - M) * LOG2E), f2_ = ex2((m2 - M) * LOG2E), f3 = ex2((m3 - M) * LOG2E);
    float o0 = fmaf(f0, asf(t[0].x), f1 * asf(t[1].x)) + fmaf(f2_, asf(t[2].x), f3 * asf(t[3].x));
    float o1 = fmaf(f0, asf(t[0].y), f1 * asf(t[1].y)) + fmaf(f2_, asf(t[2].y), f3 * asf(t[3].y));
    float ls = dpp<0xF5>(fmaf(f0, asf(t[0].z), f1 * asf(t[1].z)) + fmaf(f2_, asf(t[2].z), f3 * asf(t[3].z)));
    o0 += dpp<R8>(o0); o1 += dpp<R8>(o1); ls += dpp<R8>(ls);
    o0 = rows_add(o0); o1 = rows_add(o1); ls = rcp(rows_add(ls));
    const unsigned op = pk(o0 * ls, o1 * ls);
    unsigned oj[8];
#pragma unroll
    for (int j = 0; j < 8; ++j) oj[j] = __builtin_amdgcn_readlane(op, j);
    __builtin_amdgcn_sched_barrier(0);
    float acc[8] = {0.f, 0.f, 0.f, 0.f, 0.f, 0.f, 0.f, 0.f};
#pragma unroll
    for (int j = 0; j < 8; ++j) mac(w[j], oj[j], acc);
#pragma unroll
    for (int k = 0; k < 4; ++k) put2(rs, a_pa + k * 1024, asu(acc[2 * k]), asu(acc[2 * k + 1]), e);
    if (lane == 0) *(volatile LAS unsigned*)(L + L_FLAG) = 2u;
    STAMP(9);
    FLUSH(8, 9); FLUSH(16, 17);
    return;
  }

  __builtin_amdgcn_s_barrier();
  if (s >= NFFN) return;
  const int id = (s & 7) * 8 + g;
  const v4u xa = *(const GAS v4u*)(p.x + 8 * lane);
  const v4u wr = *(const GAS v4u*)(p.wr + 8 * lane);
  const unsigned xx = *(const GAS unsigned*)(p.x + 2 * (16 * (id & 15) + 4 * (lane & 3) + (id >> 4)));
  const v4u xb = *(const GAS v4u*)(p.x + g * 64 + 8 * rho);
  float mean, rden;
  ln_stats(xa, mean, rden);
  wait_word<4>(L + L_CNT);
  Pairs wd[4];
  unsigned gu0, gu1;
  {
    const v4u n = ln_apply(xb, mean, rden);
    float ga[8] = {0.f, 0.f, 0.f, 0.f, 0.f, 0.f, 0.f, 0.f}, ua[8] = {0.f, 0.f, 0.f, 0.f, 0.f, 0.f, 0.f, 0.f};
#pragma unroll
    for (int i = 0; i < 4; ++i) {
      const LAS unsigned char* w = L + 2 * i * 1024 + lane * 16;
      mac(pair_rows(*(const LAS v4u*)(w + L_WG), *(const LAS v4u*)(w + L_WG + 1024)), n[i], ga);
      mac(pair_rows(*(const LAS v4u*)(w + L_WU), *(const LAS v4u*)(w + L_WU + 1024)), n[i], ua);
      wd[i] = pair_rows(*(const LAS v4u*)(w + L_WD), *(const LAS v4u*)(w + L_WD + 1024));
#pragma unroll
      for (int k = 0; k < 8; ++k) pin(wd[i].c[k]);
    }
    const f2 gs = reduce8(ga), us = reduce8(ua);
    gu0 = asu((rho & 1) ? us.x : gs.x); gu1 = asu((rho & 1) ? us.y : gs.y);
  }
  const v4u nx = ln_apply(xa, mean, rden);
  const float r = rbf(wave_sum(dot2(nx.x, wr.x, dot2(nx.y, wr.y, 0.f)) + dot2(nx.z, wr.z, dot2(nx.w, wr.w, 0.f))));
  float sg = rbf(rcp(1.0f + ex2(-LOG2E * r)));
  const unsigned xnp = ln_pair(xx, mean, rden);
  float xn1 = lane < 32 ? lo(xnp) : hi(xnp), x1 = lane < 32 ? lo(xx) : hi(xx);
  int og[8], oy[2], a_py = WS_Y + s * ROW + (g * 4 + (lane >> 4)) * 128 + c * 16;
#pragma unroll
  for (int i = 0; i < 8; ++i) og[i] = WS_GU + (s * 8 + i) * 1024 + lane * 16;
  const bool two = (lane >> 2) + 16 < NFFN;
  oy[0] = WS_Y + (((id & 15) >> 1) * 4 + (id >> 4)) * 128 + (4 * (id & 1) + (lane & 3)) * 16 + (lane >> 2) * ROW;
  oy[1] = oy[0] + (two ? 16 * ROW : 0);
  pin(gu0); pin(gu1); pin(sg); pin(xn1); pin(x1); pin(oy[0]); pin(oy[1]); pin(a_py);
  wait_word<1>(L + L_FLAG);
  const v2u e = *(const LAS v2u*)(L + L_E);
#pragma unroll 1
  for (int it = 0; it < 65536 && progress(L) < GPROG; ++it) __builtin_amdgcn_s_sleep(4);
  put2(rs, WS_GU + (s * 8 + g) * 1024 + lane * 16, gu0, gu1, e);
  lds_barrier();
  v4u gu[8];
  gather<8, false>(rs, og, e, gu);
  const unsigned ap = pk(((asf(gu[0].x) + asf(gu[1].x)) + (asf(gu[2].x) + asf(gu[3].x))) + ((asf(gu[4].x) + asf(gu[5].x)) + (asf(gu[6].x) + asf(gu[7].x))),
                         ((asf(gu[0].y) + asf(gu[1].y)) + (asf(gu[2].y) + asf(gu[3].y))) + ((asf(gu[4].y) + asf(gu[5].y)) + (asf(gu[6].y) + asf(gu[7].y))));
  const float a0 = lo(ap), a1 = hi(ap);
  const unsigned sp = pk(a0 * rcp(1.0f + ex2(-LOG2E * a0)), a1 * rcp(1.0f + ex2(-LOG2E * a1)));
  const int hp = (int)pk(lo(sp) * dpp<R8>(a0), hi(sp) * dpp<R8>(a1));
  float y[8] = {0.f, 0.f, 0.f, 0.f, 0.f, 0.f, 0.f, 0.f};
#pragma unroll
  for (int i = 0; i < 4; ++i) mac(wd[i], (unsigned)__builtin_amdgcn_ds_bpermute((16 * i + rho) * 4, hp), y);
  const f2 ys = reduce8(y);
  if ((rho & 1) == 0) put2(rs, a_py, asu(ys.x), asu(ys.y), e);
  STAMP(5);
  FLUSH(5, 5);
  if (s >= 8) return;
  __builtin_amdgcn_s_sleep(DY);
  v4u yv[2];
  gather<2, false>(rs, oy, e, yv);
  const float yc = rbf(reduce_quads(asf(yv[0].x) + (two ? asf(yv[1].x) : 0.f), asf(yv[0].y) + (two ? asf(yv[1].y) : 0.f)));
  if ((lane & 28) == 0) *(LAS f2*)(L + L_B + ((lane >> 5) * 4 + (lane & 3)) * 8) = f2{rbf(xn1 + rbf(yc * sg)), x1};
  asm volatile("s_waitcnt lgkmcnt(0)" ::: "memory");
  STAMP(7);
  FLUSH(7, 7);
}

extern "C" __device__ __attribute__((used, noinline)) void END_MARK() {}

extern "C" void kernel_launch(void* const* d_in, const int* in_sizes, int n_in, void* d_out, int out_size, void* d_ws, size_t ws_size,
                              hipStream_t stream) {
  (void)in_sizes; (void)n_in; (void)out_size;
  Params p;
  p.x = (const unsigned short*)d_in[0];
  p.k = (const unsigned short*)d_in[1];
  p.v = (const unsigned short*)d_in[2];
  p.wq = (const unsigned short*)d_in[3];
  p.wo = (const unsigned short*)d_in[4];
  p.wr = (const unsigned short*)d_in[6];
  p.wg = (const unsigned short*)d_in[7];
  p.wu = (const unsigned short*)d_in[8];
  p.wd = (const unsigned short*)d_in[9];
  p.out = (unsigned*)d_out;
  p.ws = (unsigned char*)d_ws + (WSTAIL ? (ws_size - WS_SPAN) / 4096 * 4096 : 0);
  hipLaunchKernelGGL(KERNEL_NAME, dim3(NWG), dim3(NTHR), 0, stream, p);
}
```

```cpp
#define OTOUCH 1
#include <hip/hip_runtime.h>
#include <hip/hip_bf16.h>
#include <stdint.h>

#define GAS __attribute__((address_space(1)))
#define LAS __attribute__((address_space(3)))
typedef unsigned v4u __attribute__((ext_vector_type(4)));
typedef unsigned v2u __attribute__((ext_vector_type(2)));
typedef unsigned v16u __attribute__((ext_vector_type(16)));
typedef float f4 __attribute__((ext_vector_type(4)));
typedef float f2 __attribute__((ext_vector_type(2)));
typedef short s16x4 __attribute__((ext_vector_type(4)));
typedef __bf16 bf16x8 __attribute__((ext_vector_type(8)));
typedef __bf16 bf16x2 __attribute__((ext_vector_type(2)));

#define END_MARK fused_decode_code_end
#define KERNEL_NAME fused_decode_kernel
#define STR2(x) #x
#define STR(x) STR2(x)
#define NSTEP_RUN NSTEP
#define HOLDT 0
#define WSTAIL 1
#define PACE0 4
#define GAIN 7
#define UNOM 540
#define NEXCH 7
#define D1 18
#define D2 16
#define DY 10

namespace {

constexpr int D = 512, DH = 128, SK = 65536, NQ = 4096, FF = 1792;
constexpr int NWG = 256, NTHR = 448;
constexpr int KEYS_PER_WG = SK / 32;
constexpr int NSTEP = KEYS_PER_WG / 32;
constexpr int RING = 4;
constexpr int NFFN = FF / 64;
constexpr float SCALE = 0.08838834764831845f, LOG2E = 1.4426950408889634f, FMIN = -3.4028234664e38f;
constexpr unsigned SPIN_CAP = 1u << 16;
constexpr int TAU16 = 220, XSTOP = 226;

constexpr int ROW = 4096 + 128;
constexpr int WS_EPOCH = 0;
constexpr int WS_Q = 4096;
constexpr int WS_GU = WS_Q + 8 * 4 * 4096;
constexpr int WS_Y = WS_GU + NFFN * 8192;
constexpr int WS_O = WS_Y + NFFN * ROW;
constexpr int WS_A = WS_O + 256 * ROW;
constexpr int WS_PL = WS_A + 256 * ROW;
constexpr int WS_END = WS_PL + 32 * 128;
constexpr size_t WS_SPAN = 4u << 20;
static_assert(WS_END + 65536 <= WS_SPAN, "workspace map");

constexpr int L_Q = 0, L_E = 1024, L_FLAG = 1032, L_CNT = 1036, L_PROG = 1040, L_LAG = 1056, L_REF = 1072, L_B = 1088, L_FIN = 1152;
constexpr int L_ML = 1280;
constexpr int REC = 4 * 132 * 4, L_REC = 1536;
constexpr int L_WO = 16384, L_WG = 32768, L_WU = 40960, L_WD = 49152;
constexpr int L_SINK = 57344;
constexpr int LDS_BYTES = 84 * 1024;

struct Params {
  const unsigned short *x, *k, *v, *wq, *wo, *wr, *wg, *wu, *wd;
  unsigned* out;
  unsigned char* ws;
};

__device__ __forceinline__ float asf(unsigned u) { return __builtin_bit_cast(float, u); }
__device__ __forceinline__ unsigned asu(float f) { return __builtin_bit_cast(unsigned, f); }
__device__ __forceinline__ float lo(unsigned u) { return asf(u << 16); }
__device__ __forceinline__ float hi(unsigned u) { return asf(u & 0xffff0000u); }
__device__ __forceinline__ unsigned pk(float a, float b) {
  bf16x2 r = {(__bf16)a, (__bf16)b};
  return __builtin_bit_cast(unsigned, r);
}
__device__ __forceinline__ float rbf(float a) { return lo(pk(a, a)); }
__device__ __forceinline__ float rcp(float a) { return __builtin_amdgcn_rcpf(a); }
__device__ __forceinline__ float ex2(float a) { return __builtin_amdgcn_exp2f(a); }
__device__ __forceinline__ unsigned xor3(unsigned a, unsigned b, unsigned c) { return __builtin_amdgcn_bitop3_b32(a, b, c, 0x96); }
__device__ __forceinline__ unsigned or3(unsigned a, unsigned b, unsigned c) { return __builtin_amdgcn_bitop3_b32(a, b, c, 0xFE); }
__device__ __forceinline__ unsigned uni(unsigned a) { return (unsigned)__builtin_amdgcn_readfirstlane((int)a); }
__device__ __forceinline__ unsigned clock32() { return (unsigned)__builtin_amdgcn_s_memrealtime(); }
__device__ __forceinline__ float dot2(unsigned a, unsigned b, float c) {
  return __builtin_amdgcn_fdot2_f32_bf16(__builtin_bit_cast(bf16x2, a), __builtin_bit_cast(bf16x2, b), c, false);
}
template <class T> __device__ __forceinline__ void pin(T& x) { asm volatile("" : "+v"(x)); }
template <int CTRL> __device__ __forceinline__ float dpp(float x) {
  return asf(__builtin_amdgcn_mov_dpp(__builtin_bit_cast(int, x), CTRL, 0xf, 0xf, true));
}
constexpr int X1 = 0xB1, X2 = 0x4E, X7 = 0x141, R4 = 0x124, R8 = 0x128;
__device__ __forceinline__ float x32_tr(float a, float b) {
  auto s = __builtin_amdgcn_permlane32_swap(asu(a), asu(b), false, false);
  return asf(s[0]) + asf(s[1]);
}
__device__ __forceinline__ float x16_tr(float a, float b) {
  auto s = __builtin_amdgcn_permlane16_swap(asu(a), asu(b), false, false);
  return asf(s[0]) + asf(s[1]);
}
__device__ __forceinline__ float rows_add(float x) { x = x16_tr(x, x); return x32_tr(x, x); }
__device__ __forceinline__ float rows_max(float x) {
  auto s = __builtin_amdgcn_permlane16_swap(asu(x), asu(x), false, false);
  x = fmaxf(asf(s[0]), asf(s[1]));
  auto t = __builtin_amdgcn_permlane32_swap(asu(x), asu(x), false, false);
  return fmaxf(asf(t[0]), asf(t[1]));
}
__device__ __forceinline__ float wave_sum(float x) {
  x += dpp<X1>(x); x += dpp<X2>(x); x += dpp<X7>(x); x += dpp<R8>(x);
  return rows_add(x);
}
__device__ __forceinline__ void lds_barrier() {
  asm volatile("s_waitcnt lgkmcnt(0)" ::: "memory");
  __builtin_amdgcn_s_barrier();
  asm volatile("" ::: "memory");
}
template <int N> __device__ __forceinline__ void pace() {
  __builtin_amdgcn_sched_barrier(0);
  asm volatile("s_waitcnt vmcnt(%0)" :: "i"(N) : "memory");
  __builtin_amdgcn_sched_barrier(0);
}
__device__ __forceinline__ void pace_lvl(unsigned lvl) {
  __builtin_amdgcn_sched_barrier(0);
  asm volatile("s_cmp_gt_u32 %0, 3\n\ts_cbranch_scc1 4f\n\ts_cmp_gt_u32 %0, 1\n\ts_cbranch_scc1 2f\n\t"
               "s_cmp_eq_u32 %0, 1\n\ts_cbranch_scc1 1f\n\ts_waitcnt vmcnt(2)\n1:\n\ts_waitcnt vmcnt(3)\n\ts_branch 9f\n"
               "2:\n\ts_cmp_eq_u32 %0, 3\n\ts_cbranch_scc1 3f\n\ts_waitcnt vmcnt(4)\n3:\n\ts_waitcnt vmcnt(5)\n\ts_branch 9f\n"
               "4:\n\ts_cmp_gt_u32 %0, 5\n\ts_cbranch_scc1 6f\n\t"
               "s_cmp_eq_u32 %0, 5\n\ts_cbranch_scc1 5f\n\ts_waitcnt vmcnt(6)\n5:\n\ts_waitcnt vmcnt(7)\n\ts_branch 9f\n"
               "6:\n\ts_cmp_gt_u32 %0, 7\n\ts_cbranch_scc1 9f\n\t"
               "s_cmp_eq_u32 %0, 7\n\ts_cbranch_scc1 7f\n\ts_waitcnt vmcnt(8)\n7:\n\ts_waitcnt vmcnt(10)\n9:"
               :: "s"(lvl) : "scc");
  __builtin_amdgcn_sched_barrier(0);
}
constexpr int SC1 = 16, NT = 2;
__device__ __forceinline__ void put2(__amdgpu_buffer_rsrc_t rs, int off, unsigned a, unsigned b, v2u e) {
  __builtin_amdgcn_raw_buffer_store_b128(v4u{a, b, a ^ e.x, b ^ e.y}, rs, off, 0, SC1);
}
__device__ __forceinline__ void put3(__amdgpu_buffer_rsrc_t rs, int off, unsigned a, unsigned b, unsigned c, v2u e) {
  __builtin_amdgcn_raw_buffer_store_b128(v4u{a, b, c, xor3(a, b, c) ^ e.x}, rs, off, 0, SC1);
}
template <int N, bool THREE> __device__ __forceinline__ void gather(__amdgpu_buffer_rsrc_t rs, const int (&off)[N], v2u e, v4u (&g)[N]) {
#pragma unroll 1
  for (unsigned spin = 0; spin < SPIN_CAP; ++spin) {
    asm volatile("" ::: "memory");
#pragma unroll
    for (int i = 0; i < N; ++i) g[i] = __builtin_amdgcn_raw_buffer_load_b128(rs, off[i], 0, SC1);
    unsigned bad = 0;
#pragma unroll
    for (int i = 0; i < N; ++i) {
      if (THREE) bad |= xor3(xor3(g[i].x, g[i].y, g[i].z), g[i].w, e.x);
      else bad = or3(xor3(g[i].x, g[i].z, e.x), xor3(g[i].y, g[i].w, e.y), bad);
    }
    if (__builtin_amdgcn_ballot_w64(bad != 0u) == 0ull) break;
  }
}
template <unsigned V> __device__ __forceinline__ void wait_word(LAS unsigned char* w) {
#pragma unroll 1
  for (unsigned spin = 0; spin < (1u << 21); ++spin) {
    if (uni(*(volatile LAS unsigned*)w) >= V) break;
    __builtin_amdgcn_s_sleep(1);
  }
  asm volatile("" ::: "memory");
}
__device__ __forceinline__ unsigned progress(LAS unsigned char* L) {
  const v4u pr = *(volatile LAS v4u*)(L + L_PROG);
  return uni(pr.x + pr.y + pr.z + pr.w);
}
__device__ __forceinline__ void dma(const void* src, unsigned dst) {
  unsigned keep;
  asm volatile("s_mov_b32 %0, m0\n\ts_mov_b32 m0, %2\n\ts_nop 0\n\tglobal_load_lds_dwordx4 %1, off nt\n\ts_mov_b32 m0, %0"
               : "=&s"(keep) : "v"(src), "s"(dst) : "memory");
}
__device__ __forceinline__ void ln_stats(v4u xa, float& mean, float& rden) {
  const unsigned one = 0x3f803f80u;
  const float mu = wave_sum(dot2(xa.x, one, dot2(xa.y, one, 0.f)) + dot2(xa.z, one, dot2(xa.w, one, 0.f))) * (1.0f / D);
  float q0 = 0.f, q1 = 0.f;
#pragma unroll
  for (int j = 0; j < 4; ++j) { const float a = lo(xa[j]) - mu, b = hi(xa[j]) - mu; q0 = fmaf(a, a, q0); q1 = fmaf(b, b, q1); }
  const float var = rbf(wave_sum(q0 + q1) * (1.0f / D));
  mean = rbf(mu);
  rden = rcp(rbf(__builtin_amdgcn_sqrtf(rbf(var + 1e-5f))));
}
__device__ __forceinline__ unsigned ln_pair(unsigned xx, float mean, float rden) {
  const unsigned d = pk(lo(xx) - mean, hi(xx) - mean);
  return pk(lo(d) * rden, hi(d) * rden);
}
__device__ __forceinline__ v4u ln_apply(v4u xa, float mean, float rden) {
  return v4u{ln_pair(xa.x, mean, rden), ln_pair(xa.y, mean, rden), ln_pair(xa.z, mean, rden), ln_pair(xa.w, mean, rden)};
}
struct Pairs { unsigned c[8]; };
__device__ __forceinline__ Pairs pair_rows(v4u w0, v4u w1) {
  Pairs r;
#pragma unroll
  for (int k = 0; k < 4; ++k) {
    r.c[2 * k] = __builtin_amdgcn_perm(w1[k], w0[k], 0x05040100u);
    r.c[2 * k + 1] = __builtin_amdgcn_perm(w1[k], w0[k], 0x07060302u);
  }
  return r;
}
__device__ __forceinline__ void mac(const Pairs& w, unsigned x, float (&acc)[8]) {
#pragma unroll
  for (int k = 0; k < 8; ++k) acc[k] = dot2(w.c[k], x, acc[k]);
}
__device__ __forceinline__ f2 reduce8(const float (&acc)[8]) {
  const float t0 = x32_tr(acc[0], acc[4]), t1 = x32_tr(acc[1], acc[5]), t2 = x32_tr(acc[2], acc[6]), t3 = x32_tr(acc[3], acc[7]);
  const float u0 = x16_tr(t0, t2), u1 = x16_tr(t1, t3);
  return f2{u0 + dpp<R8>(u0), u1 + dpp<R8>(u1)};
}
__device__ __forceinline__ float reduce_quads(float a, float b) {
  float t = x32_tr(a, b);
  t = x16_tr(t, t);
  t += dpp<R4>(t);
  return t + dpp<R8>(t);
}
#define STAMP(i) do {} while (0)
#define FLUSH(a, b) do {} while (0)

struct Acc { f4 o0, o1; float m, l; };

__device__ __forceinline__ unsigned scores_to_p(v4u ka, v4u kb, bf16x8 qf, Acc& s) {
  const f4 z = {0.f, 0.f, 0.f, 0.f};
  const f4 da = __builtin_amdgcn_mfma_f32_16x16x32_bf16(__builtin_bit_cast(bf16x8, ka), qf, z, 0, 0, 0);
  const f4 db = __builtin_amdgcn_mfma_f32_16x16x32_bf16(__builtin_bit_cast(bf16x8, kb), qf, z, 0, 0, 0);
  float sa = (dpp<0x00>(da[0]) + dpp<0x55>(da[1])) + (dpp<0xAA>(da[2]) + dpp<0xFF>(da[3]));
  float sb = (dpp<0x00>(db[0]) + dpp<0x55>(db[1])) + (dpp<0xAA>(db[2]) + dpp<0xFF>(db[3]));
  unsigned u = pk(sa, sb);
  u = pk(lo(u) * SCALE, hi(u) * SCALE);
  sa = lo(u); sb = hi(u);
  float mx = fmaxf(sa, sb);
  if (__builtin_amdgcn_ballot_w64(mx > s.m) != 0ull) {
    mx = fmaxf(s.m, rows_max(mx));
    const float al = ex2((s.m - mx) * LOG2E);
    s.o0 *= al; s.o1 *= al; s.l *= al; s.m = mx;
  }
  const float pa = ex2((sa - s.m) * LOG2E), pb = ex2((sb - s.m) * LOG2E);
  s.l += pa + pb;
  return pk(pa, pb);
}
__device__ __forceinline__ void pv(v4u va, v4u vb, unsigned pp, v4u sel, Acc& s) {
  const v2u ba = {__builtin_amdgcn_perm(pp, pp, sel.x), __builtin_amdgcn_perm(pp, pp, sel.y)};
  const v2u bb = {__builtin_amdgcn_perm(pp, pp, sel.z), __builtin_amdgcn_perm(pp, pp, sel.w)};
  s.o0 = __builtin_amdgcn_mfma_f32_16x16x16bf16_1k(__builtin_bit_cast(s16x4, v2u{va.x, va.y}), __builtin_bit_cast(s16x4, ba), s.o0, 0, 0, 0);
  s.o1 = __builtin_amdgcn_mfma_f32_16x16x16bf16_1k(__builtin_bit_cast(s16x4, v2u{va.z, va.w}), __builtin_bit_cast(s16x4, ba), s.o1, 0, 0, 0);
  s.o0 = __builtin_amdgcn_mfma_f32_16x16x16bf16_1k(__builtin_bit_cast(s16x4, v2u{vb.x, vb.y}), __builtin_bit_cast(s16x4, bb), s.o0, 0, 0, 0);
  s.o1 = __builtin_amdgcn_mfma_f32_16x16x16bf16_1k(__builtin_bit_cast(s16x4, v2u{vb.z, vb.w}), __builtin_bit_cast(s16x4, bb), s.o1, 0, 0, 0);
}

}

extern "C" __global__ void __launch_bounds__(NTHR, 2) KERNEL_NAME(Params) {
  __shared__ __attribute__((aligned(16))) unsigned char smem[LDS_BYTES];
  LAS unsigned char* L = (LAS unsigned char*)smem;
  v16u arg0; v4u arg1; v2u arg2;
  int wave;
  {
    unsigned long long pc, wo;
    unsigned t0, t1, m0s, v0, v1;
    asm volatile("s_load_dwordx16 %0, %11, 0x0\n\ts_load_dwordx4 %1, %11, 0x40\n\ts_load_dwordx2 %2, %11, 0x50\n\ts_load_dwordx2 %4, %11, 0x20\n\t"
                 "v_readfirstlane_b32 %5, %12\n\tv_and_b32 %9, 63, %12\n\ts_mov_b32 %8, m0\n\ts_lshr_b32 %5, %5, 6\n\t"
                 "v_lshlrev_b32 %9, 4, %9\n\ts_lshr_b32 %6, %13, 3\n\ts_cmp_lg_u32 %5, " "5" "\n\ts_cbranch_scc1 1f\n\t"
                 ".p2align 4\n\tv_lshl_or_b32 %10, %6, 10, %9\n\ts_nop 0\n\ts_getpc_b64 %3\n\ts_mov_b32 %7, " STR(END_MARK) "@rel32@lo-12\n\t"
                 "s_and_b32 %7, %7, -16\n\ts_cmpk_gt_u32 %7, 0x7ff0\n\ts_cbranch_scc1 1f\n\t"
                 "s_mov_b32 m0, %14\n\tv_min_u32 %10, %7, %10\n\tglobal_load_lds_dwordx4 %10, %3\n"
                 "1:\n\ts_lshl_b32 %7, %5, 12\n\ts_add_u32 m0, %7, %15\n\ts_lshl_b32 %6, %6, 2\n\ts_add_u32 %6, %6, %5\n\t"
                 "s_and_b32 %7, %13, 7\n\ts_xor_b32 %7, %7, 1\n\ts_lshl_b32 %7, %7, 7\n\ts_add_u32 %6, %6, %7\n\t"
                 "v_lshl_or_b32 %10, %6, 12, %9\n\ts_cmp_gt_u32 %5, 3\n\ts_waitcnt lgkmcnt(0)\n\ts_cbranch_scc1 2f\n\t"
                 "global_load_lds_dwordx4 %10, %4 nt\n\tglobal_load_lds_dwordx4 %10, %4 offset:1024 nt\n\t"
                 "global_load_lds_dwordx4 %10, %4 offset:2048 nt\n\tglobal_load_lds_dwordx4 %10, %4 offset:3072 nt\n"
                 "2:\n\ts_mov_b32 m0, %8"
                 : "=&s"(arg0), "=&s"(arg1), "=&s"(arg2), "=&s"(pc), "=&s"(wo), "=&s"(wave), "=&s"(t0), "=&s"(t1), "=&s"(m0s), "=&v"(v0), "=&v"(v1)
                 : "s"(__builtin_amdgcn_kernarg_segment_ptr()), "v"(threadIdx.x), "s"(blockIdx.x), "i"(L_SINK), "i"(L_WO)
                 : "scc", "memory");
  }
  const int lane = threadIdx.x & 63;
  const int b = blockIdx.x, g = b & 7, s = b >> 3;
  Params p;
  {
    auto ptr = [](unsigned l, unsigned h) { return (const unsigned short*)(const GAS unsigned short*)(((unsigned long long)h << 32) | l); };
    p.x = ptr(arg0[0], arg0[1]); p.k = ptr(arg0[2], arg0[3]); p.v = ptr(arg0[4], arg0[5]); p.wq = ptr(arg0[6], arg0[7]);
    p.wo = ptr(arg0[8], arg0[9]); p.wr = ptr(arg0[10], arg0[11]); p.wg = ptr(arg0[12], arg0[13]); p.wu = ptr(arg0[14], arg0[15]);
    p.wd = ptr(arg1[0], arg1[1]); p.out = (unsigned*)ptr(arg1[2], arg1[3]); p.ws = (unsigned char*)ptr(arg2[0], arg2[1]);
  }
  const int gx = g ^ 1;
  const unsigned hold0 = clock32();
  const __amdgpu_buffer_rsrc_t rs = __builtin_amdgcn_make_buffer_rsrc((void*)p.ws, 0, WS_END, 0x00020000);

  if (wave < 4) {
    const size_t base = ((size_t)g * SK + (size_t)s * KEYS_PER_WG) * DH;
    const __amdgpu_buffer_rsrc_t rk = __builtin_amdgcn_make_buffer_rsrc((void*)(p.k + base), 0, KEYS_PER_WG * DH * 2, 0x00020000);
    const __amdgpu_buffer_rsrc_t rv = __builtin_amdgcn_make_buffer_rsrc((void*)(p.v + base), 0, KEYS_PER_WG * DH * 2, 0x00020000);
    const int h = (lane >> 2) & 3, c = lane & 3;
    const int frag = h * 256 + (lane >> 4) * 64 + c * 16;
    int ko = wave * 2048 + frag, vo = wave * 2048 + lane * 16;
    constexpr int STRIDE = 32 * DH * 2;
    if (threadIdx.x == 0) {
      *(volatile LAS v2u*)(L + L_FLAG) = v2u{0u, 0u};
      *(volatile LAS v4u*)(L + L_PROG) = v4u{0u, 0u, 0u, 0u};
      *(volatile LAS v2u*)(L + L_REF) = v2u{0u, 0u};
    }
    const unsigned lb = (unsigned)(size_t)L;
    const int row = 8 * (lane >> 3) + 2 * wave;
    const unsigned short* wg = p.wg + (size_t)(g * 64 + row) * FF + s * 64 + (lane & 7) * 8;
    const unsigned short* wu = p.wu + (size_t)(g * 64 + row) * FF + s * 64 + (lane & 7) * 8;
    const unsigned short* wd = p.wd + (size_t)(s * 64 + row) * D + g * 64 + (lane & 7) * 8;
    pin(wg); pin(wu); pin(wd); pin(ko); pin(vo);
    lds_barrier();
    v4u ka[RING], kb[RING], va[RING], vb[RING];
    if (s < NFFN) {
      dma(wg, lb + L_WG + 2 * wave * 1024);
      dma(wu, lb + L_WU + 2 * wave * 1024);
      pace<PACE0>();
      dma(wg + FF, lb + L_WG + (2 * wave + 1) * 1024);
      dma(wu + FF, lb + L_WU + (2 * wave + 1) * 1024);
      pace<PACE0>();
      dma(wd, lb + L_WD + 2 * wave * 1024);
      dma(wd + D, lb + L_WD + (2 * wave + 1) * 1024);
    }
#pragma unroll
    for (int r = 0; r < RING; ++r) {
      pace<PACE0>();
      if (r == (PACE0 + 3) / 4 && lane == 0)
        __hip_atomic_fetch_add((LAS unsigned*)(L + L_CNT), 1u, __ATOMIC_RELAXED, __HIP_MEMORY_SCOPE_WORKGROUP);
      ka[r] = __builtin_amdgcn_raw_buffer_load_b128(rk, ko, 0, NT);
      kb[r] = __builtin_amdgcn_raw_buffer_load_b128(rk, ko + 1024, 0, NT);
      pace<PACE0>();
      va[r] = __builtin_amdgcn_raw_buffer_load_b128(rv, vo, 0, NT);
      vb[r] = __builtin_amdgcn_raw_buffer_load_b128(rv, vo + 1024, 0, NT);
      ko += STRIDE; vo += STRIDE;
    }
    __builtin_amdgcn_sched_barrier(0);
    const unsigned z = 0x0c0c0c0cu;
    const v4u sel = {c == 0 ? 0x0c0c0100u : c == 1 ? 0x01000c0cu : z, c == 2 ? 0x0c0c0100u : c == 3 ? 0x01000c0cu : z,
                     c == 0 ? 0x0c0c0302u : c == 1 ? 0x03020c0cu : z, c == 2 ? 0x0c0c0302u : c == 3 ? 0x03020c0cu : z};
    int a_ml = L_ML + h * 32 + wave * 8, a_ow = L_REC + wave * REC + (h * 132 + (lane >> 4) * 32 + c) * 4, a_or = L_REC + (wave * 132 + 2 * lane) * 4;
    int a_po = WS_O + (g * 32 + s) * ROW + threadIdx.x * 16;
    pin(a_ml); pin(a_ow); pin(a_or); pin(a_po);
    STAMP(14);
    wait_word<1>(L + L_FLAG);
    const bf16x8 qf = __builtin_bit_cast(bf16x8, *(const LAS v4u*)(L + L_Q + frag));
    v2u e = *(const LAS v2u*)(L + L_E);
    pin(e);
    STAMP(1);
    Acc a;
    a.o0 = f4{0.f, 0.f, 0.f, 0.f}; a.o1 = a.o0; a.m = FMIN; a.l = 0.f;
#pragma unroll 1
    for (int i = NSTEP_RUN - RING; i >= 0; i -= RING) {
#pragma unroll
      for (int r = 0; r < RING; ++r) {
        const int n = NSTEP - RING + r - i;
        const v2u ref = *(volatile LAS v2u*)(L + L_REF);
        const unsigned lag = clock32() - (n * (4 * TAU16) >> 4);
        *(volatile LAS unsigned*)(L + L_PROG + 4 * wave) = (unsigned)n;
        *(volatile LAS unsigned*)(L + L_LAG + 4 * wave) = lag;
        const unsigned pp = scores_to_p(ka[r], kb[r], qf, a);
        int lv = (UNOM - (int)((uni(ref.x) - lag) & uni(ref.y)) * GAIN) >> 8;
        asm("s_max_i32 %0, %0, 0\n\ts_min_i32 %0, %0, 7" : "+s"(lv) :: "scc");
        lv = i ? lv : 8;
        pace_lvl(lv);
        ka[r] = __builtin_amdgcn_raw_buffer_load_b128(rk, ko, 0, NT);
        kb[r] = __builtin_amdgcn_raw_buffer_load_b128(rk, ko + 1024, 0, NT);
        if (r == RING - 1) *(volatile LAS f2*)(L + a_ml) = f2{a.m, rows_add(a.l)};
        __builtin_amdgcn_sched_barrier(0);
        pv(va[r], vb[r], pp, sel, a);
        pace_lvl(lv);
        va[r] = __builtin_amdgcn_raw_buffer_load_b128(rv, vo, 0, NT);
        vb[r] = __builtin_amdgcn_raw_buffer_load_b128(rv, vo + 1024, 0, NT);
        ko += STRIDE; vo += STRIDE;
        __builtin_amdgcn_sched_barrier(0);
      }
    }
    STAMP(2);
#pragma unroll
    for (int i = 0; i < 4; ++i) { *(LAS float*)(L + a_ow + 32 * i) = a.o0[i]; *(LAS float*)(L + a_ow + 32 * i + 16) = a.o1[i]; }
    lds_barrier();
    {
      const f4 ml0 = *(const LAS f4*)(L + L_ML + wave * 32), ml1 = *(const LAS f4*)(L + L_ML + wave * 32 + 16);
      const f2 t0 = *(const LAS f2*)(L + a_or), t1 = *(const LAS f2*)(L + a_or + REC), t2 = *(const LAS f2*)(L + a_or + 2 * REC), t3 = *(const LAS f2*)(L + a_or + 3 * REC);
      __builtin_amdgcn_sched_barrier(0);
      const float M = fmaxf(fmaxf(ml0.x, ml0.z), fmaxf(ml1.x, ml1.z));
      const float f0 = ex2((ml0.x - M) * LOG2E), f1 = ex2((ml0.z - M) * LOG2E), f2_ = ex2((ml1.x - M) * LOG2E), f3 = ex2((ml1.z - M) * LOG2E);
      const f2 o = (f0 * t0 + f1 * t1) + (f2_ * t2 + f3 * t3);
      const float ls = fmaf(f0, ml0.y, f1 * ml0.w) + fmaf(f2_, ml1.y, f3 * ml1.w);
      put3(rs, a_po, asu(o.x), asu(o.y), asu((lane & 1) ? ls : M), e);
    }
    STAMP(3);
    if (wave == 0) { FLUSH(0, 3); FLUSH(14, 14); }
    if (s >= 8) return;
    const int sl = 4 * (g * 8 + s) + c;
    int off[4];
#pragma unroll
    for (int i = 0; i < 4; ++i) { off[i] = WS_A + (64 * wave + 16 * i + (lane >> 2)) * ROW + sl * 16; pin(off[i]); }
    GAS unsigned short* po = (GAS unsigned short*)p.out + 2 * (4 * (sl & 63) + (sl >> 6)) + ((lane >> 2) & 1);
    int a_fw = L_FIN + wave * 32 + (lane >> 5) * 16 + c * 4;
    pin(po); pin(a_fw);
    wait_word<2>(L + L_FLAG);
    __builtin_amdgcn_s_sleep(D2);
    v4u t[4];
    gather<4, false>(rs, off, e, t);
    STAMP(10);
    const float av = reduce_quads((asf(t[0].x) + asf(t[1].x)) + (asf(t[2].x) + asf(t[3].x)), (asf(t[0].y) + asf(t[1].y)) + (asf(t[2].y) + asf(t[3].y)));
    if ((lane & 28) == 0) *(LAS float*)(L + a_fw) = av;
    lds_barrier();
    if (wave == 0 && lane < 8) {
      const LAS float* fin = (const LAS float*)(L + L_FIN) + lane;
      const f2 bx = *(const LAS f2*)(L + L_B + lane * 8);
      const float r = rbf((fin[0] + fin[8]) + (fin[16] + fin[24]));
      for (int i = 0; i < 400000 && (int)(clock32() - hold0) < 6000; ++i) {}
      *po = (unsigned short)pk(rbf(bx.y + r) + bx.x, 0.f);
      if (b == 0 && lane == 0) __builtin_amdgcn_raw_buffer_store_b64(e, rs, WS_EPOCH, 0, SC1);
    }
    STAMP(11);
    if (wave == 0) FLUSH(10, 11);
    return;
  }

  const int rho = lane >> 3, c = lane & 7;
  if (wave == 4) {
    const int kq = s & 3, strip = s >> 2;
    const int r0 = kq * 128 + 16 * rho;
    const v4u xa = *(const GAS v4u*)(p.x + 8 * lane);
    const v4u xb0 = *(const GAS v4u*)(p.x + r0), xb1 = *(const GAS v4u*)(p.x + r0 + 8);
    const v2u eo = __builtin_amdgcn_raw_buffer_load_b64(rs, WS_EPOCH, 0, SC1);
    const __amdgpu_buffer_rsrc_t rq = __builtin_amdgcn_make_buffer_rsrc((void*)p.wq, 0, D * NQ * 2, 0x00020000);
    const int qo = (r0 * NQ + gx * 512 + strip * 64 + c * 8) * 2;
    v4u w[16];
#pragma unroll
    for (int i = 0; i < 16; ++i) w[i] = __builtin_amdgcn_raw_buffer_load_b128(rq, qo, i * NQ * 2, NT);
    __builtin_amdgcn_sched_barrier(0);
    __builtin_amdgcn_s_barrier();
    STAMP(12);
    float mean, rden;
    ln_stats(xa, mean, rden);
    const v4u n0 = ln_apply(xb0, mean, rden), n1 = ln_apply(xb1, mean, rden);
    v2u e = {uni(eo.x) + 1u, uni(eo.y) + 1u};
    if (e.x == 0u) e.x = 1u;
    if (e.y == 0u) e.y = 1u;
    float acc[8] = {0.f, 0.f, 0.f, 0.f, 0.f, 0.f, 0.f, 0.f};
#pragma unroll
    for (int i = 0; i < 4; ++i) mac(pair_rows(w[2 * i], w[2 * i + 1]), n0[i], acc);
#pragma unroll
    for (int i = 0; i < 4; ++i) mac(pair_rows(w[8 + 2 * i], w[9 + 2 * i]), n1[i], acc);
    const f2 u = reduce8(acc);
    if ((rho & 1) == 0) put2(rs, WS_Q + (gx * 4 + kq) * 4096 + (strip * 32 + (lane >> 4) * 8 + c) * 16, asu(u.x), asu(u.y), e);
    STAMP(4);
    int off[16];
#pragma unroll
    for (int i = 0; i < 16; ++i) off[i] = WS_Q + (g * 4 + (i & 3)) * 4096 + ((i >> 2) * 64 + lane) * 16;
    v4u q[16];
    gather<16, false>(rs, off, e, q);
#pragma unroll
    for (int i = 0; i < 4; ++i)
      *(LAS unsigned*)(L + L_Q + (i * 64 + (lane & 32) + 4 * c + (rho & 3)) * 4) =
          pk((asf(q[4 * i].x) + asf(q[4 * i + 1].x)) + (asf(q[4 * i + 2].x) + asf(q[4 * i + 3].x)),
             (asf(q[4 * i].y) + asf(q[4 * i + 1].y)) + (asf(q[4 * i + 2].y) + asf(q[4 * i + 3].y)));
    if (lane == 0) *(LAS v2u*)(L + L_E) = e;
    asm volatile("s_waitcnt lgkmcnt(0)" ::: "memory");
    if (lane == 0) *(volatile LAS unsigned*)(L + L_FLAG) = 1u;
    STAMP(6);
    FLUSH(4, 4); FLUSH(6, 6); FLUSH(12, 12);
    const bool rd = lane < 8 && lane != g;
    unsigned last = 0, t_ref = 0, ref = 0, t_first = 0, ref_first = 0;
    float drift = 0.f;
    int nref = 0;
#pragma unroll 1
    for (int it = 0; it < 8192 && last < 4 * (NSTEP - RING); ++it) {
      const bool exch = it % NEXCH == 0 && last < XSTOP;
      v4u t = {0u, 0u, 0u, 0u};
      asm volatile("" ::: "memory");
      if (exch && it && rd) t = __builtin_amdgcn_raw_buffer_load_b128(rs, WS_PL + (((s + 5 * (it / NEXCH) + 3 * lane) & 24) + lane) * 128, 0, SC1);
      asm volatile("" : "+v"(t) :: "memory");
      const v4u pr = *(volatile LAS v4u*)(L + L_PROG), lg = *(volatile LAS v4u*)(L + L_LAG);
      const unsigned now = clock32();
      last = uni(pr.x + pr.y + pr.z + pr.w);
      const bool begun = uni(min(min(pr.x, pr.y), min(pr.z, pr.w))) != 0u;
      const unsigned lag = begun ? uni(lg.x + (unsigned)(((int)(lg.y - lg.x) + (int)(lg.z - lg.x) + (int)(lg.w - lg.x)) >> 2)) : now - (last * TAU16 >> 4);
      if (exch) {
        if (lane == 0 && (s & 7) == 0) put2(rs, WS_PL + (s + g) * 128, lag, now, e);
        const bool ok = rd && (t.x ^ t.z) == e.x && (t.y ^ t.w) == e.y;
        float sl = ok ? (float)(int)(t.x - lag) : 0.f, st = ok ? (float)(int)(t.y - now) : 0.f;
        sl += dpp<X1>(sl); st += dpp<X1>(st); sl += dpp<X2>(sl); st += dpp<X2>(st); sl += dpp<X7>(sl); st += dpp<X7>(st);
        const int n = __builtin_popcountll(__builtin_amdgcn_ballot_w64(ok));
        if (n >= 4) {
          const float w = rcp((float)(n + 1));
          ref = lag + uni((unsigned)(int)(sl * w));
          t_ref = now + uni((unsigned)(int)(st * w));
          if (nref == 1) { ref_first = ref; t_first = t_ref; }
          else if (nref > 1) drift = (float)(int)(ref - ref_first) * rcp((float)(int)(t_ref - t_first) + 200.f);
          ++nref;
        }
      }
      if (lane == 0 && begun) *(volatile LAS v2u*)(L + L_REF) = v2u{nref ? ref + (unsigned)(int)(drift * (float)(int)(now - t_ref)) : lag, ~0u};
      __builtin_amdgcn_s_sleep(10);
    }
    return;
  }

  if (wave == 5) {
    __builtin_amdgcn_s_barrier();
    wait_word<4>(L + L_CNT);
    Pairs w[8];
#pragma unroll
    for (int j = 0; j < 8; ++j) {
      w[j] = pair_rows(*(const LAS v4u*)(L + L_WO + 2 * j * 1024 + lane * 16), *(const LAS v4u*)(L + L_WO + (2 * j + 1) * 1024 + lane * 16));
#pragma unroll
      for (int k = 0; k < 8; ++k) pin(w[j].c[k]);
    }
    int off[4], a_pa = WS_A + (g * 32 + s) * ROW + lane * 16;
#pragma unroll
    for (int i = 0; i < 4; ++i) { off[i] = WS_O + (gx * 32 + rho + 8 * i) * ROW + s * 128 + c * 16; pin(off[i]); }
    pin(a_pa);
    lds_barrier();
    const v2u e = *(const LAS v2u*)(L + L_E);
    STAMP(16);
    __builtin_amdgcn_s_sleep(D1);
    v4u t[4];
    gather<4, true>(rs, off, e, t);
    STAMP(8);
    const float m0 = dpp<0xA0>(asf(t[0].z)), m1 = dpp<0xA0>(asf(t[1].z)), m2 = dpp<0xA0>(asf(t[2].z)), m3 = dpp<0xA0>(asf(t[3].z));
    float M = fmaxf(fmaxf(m0, m1), fmaxf(m2, m3));
    M = rows_max(fmaxf(M, dpp<R8>(M)));
    const float f0 = ex2((m0 - M) * LOG2E), f1 = ex2((m1 - M) * LOG2E), f2_ = ex2((m2 - M) * LOG2E), f3 = ex2((m3 - M) * LOG2E);
    float o0 = fmaf(f0, asf(t[0].x), f1 * asf(t[1].x)) + fmaf(f2_, asf(t[2].x), f3 * asf(t[3].x));
    float o1 = fmaf(f0, asf(t[0].y), f1 * asf(t[1].y)) + fmaf(f2_, asf(t[2].y), f3 * asf(t[3].y));
    float ls = dpp<0xF5>(fmaf(f0, asf(t[0].z), f1 * asf(t[1].z)) + fmaf(f2_, asf(t[2].z), f3 * asf(t[3].z)));
    o0 += dpp<R8>(o0); o1 += dpp<R8>(o1); ls += dpp<R8>(ls);
    o0 = rows_add(o0); o1 = rows_add(o1); ls = rcp(rows_add(ls));
    const unsigned op = pk(o0 * ls, o1 * ls);
    unsigned oj[8];
#pragma unroll
    for (int j = 0; j < 8; ++j) oj[j] = __builtin_amdgcn_readlane(op, j);
    __builtin_amdgcn_sched_barrier(0);
    float acc[8] = {0.f, 0.f, 0.f, 0.f, 0.f, 0.f, 0.f, 0.f};
#pragma unroll
    for (int j = 0; j < 8; ++j) mac(w[j], oj[j], acc);
#pragma unroll
    for (int k = 0; k < 4; ++k) put2(rs, a_pa + k * 1024, asu(acc[2 * k]), asu(acc[2 * k + 1]), e);
    if (lane == 0) *(volatile LAS unsigned*)(L + L_FLAG) = 2u;
    STAMP(9);
    FLUSH(8, 9); FLUSH(16, 16);
    return;
  }

  __builtin_amdgcn_s_barrier();
  if (s >= NFFN) return;
  const int sl = 4 * (g * 8 + (s & 7)) + (lane & 3), cp = 4 * (sl & 63) + (sl >> 6);
  const v4u xa = *(const GAS v4u*)(p.x + 8 * lane);
  const v4u wr = *(const GAS v4u*)(p.wr + 8 * lane);
  const unsigned xx = *(const GAS unsigned*)(p.x + 2 * cp);
  const v4u xb = *(const GAS v4u*)(p.x + g * 64 + 8 * rho);
  if (OTOUCH && s < 8) { const unsigned t = __builtin_nontemporal_load((const GAS unsigned*)p.out + cp); asm volatile("" :: "v"(t)); }
  float mean, rden;
  ln_stats(xa, mean, rden);
  wait_word<4>(L + L_CNT);
  Pairs wd[4];
  unsigned gu0, gu1;
  {
    const v4u n = ln_apply(xb, mean, rden);
    float ga[8] = {0.f, 0.f, 0.f, 0.f, 0.f, 0.f, 0.f, 0.f}, ua[8] = {0.f, 0.f, 0.f, 0.f, 0.f, 0.f, 0.f, 0.f};
#pragma unroll
    for (int i = 0; i < 4; ++i) {
      const LAS unsigned char* w = L + 2 * i * 1024 + lane * 16;
      mac(pair_rows(*(const LAS v4u*)(w + L_WG), *(const LAS v4u*)(w + L_WG + 1024)), n[i], ga);
      mac(pair_rows(*(const LAS v4u*)(w + L_WU), *(const LAS v4u*)(w + L_WU + 1024)), n[i], ua);
      wd[i] = pair_rows(*(const LAS v4u*)(w + L_WD), *(const LAS v4u*)(w + L_WD + 1024));
#pragma unroll
      for (int k = 0; k < 8; ++k) pin(wd[i].c[k]);
    }
    const f2 gs = reduce8(ga), us = reduce8(ua);
    gu0 = asu((rho & 1) ? us.x : gs.x); gu1 = asu((rho & 1) ? us.y : gs.y);
  }
  const v4u nx = ln_apply(xa, mean, rden);
  const float r = rbf(wave_sum(dot2(nx.x, wr.x, dot2(nx.y, wr.y, 0.f)) + dot2(nx.z, wr.z, dot2(nx.w, wr.w, 0.f))));
  float sg = rbf(rcp(1.0f + ex2(-LOG2E * r)));
  const unsigned xnp = ln_pair(xx, mean, rden);
  float xn1 = lane < 32 ? lo(xnp) : hi(xnp), x1 = lane < 32 ? lo(xx) : hi(xx);
  int og[8], oy[2], a_py = WS_Y + s * ROW + (g * 4 + (lane >> 4)) * 128 + c * 16;
#pragma unroll
  for (int i = 0; i < 8; ++i) og[i] = WS_GU + (s * 8 + i) * 1024 + lane * 16;
  const bool two = (lane >> 2) + 16 < NFFN;
  oy[0] = WS_Y + ((cp >> 5) * 4 + (cp & 3)) * 128 + ((cp >> 2) & 7) * 16 + (lane >> 2) * ROW;
  oy[1] = oy[0] + (two ? 16 * ROW : 0);
  pin(oy[0]); pin(oy[1]);
  pin(gu0); pin(gu1); pin(sg); pin(xn1); pin(x1); pin(a_py);
  wait_word<1>(L + L_FLAG);
  const v2u e = *(const LAS v2u*)(L + L_E);
#pragma unroll 1
  for (int it = 0; it < 65536 && progress(L) < 4 * (NSTEP - RING); ++it) __builtin_amdgcn_s_sleep(4);
  put2(rs, WS_GU + (s * 8 + g) * 1024 + lane * 16, gu0, gu1, e);
  lds_barrier();
  v4u gu[8];
  gather<8, false>(rs, og, e, gu);
  const unsigned ap = pk(((asf(gu[0].x) + asf(gu[1].x)) + (asf(gu[2].x) + asf(gu[3].x))) + ((asf(gu[4].x) + asf(gu[5].x)) + (asf(gu[6].x) + asf(gu[7].x))),
                         ((asf(gu[0].y) + asf(gu[1].y)) + (asf(gu[2].y) + asf(gu[3].y))) + ((asf(gu[4].y) + asf(gu[5].y)) + (asf(gu[6].y) + asf(gu[7].y))));
  const float a0 = lo(ap), a1 = hi(ap);
  const unsigned sp = pk(a0 * rcp(1.0f + ex2(-LOG2E * a0)), a1 * rcp(1.0f + ex2(-LOG2E * a1)));
  const int hp = (int)pk(lo(sp) * dpp<R8>(a0), hi(sp) * dpp<R8>(a1));
  float y[8] = {0.f, 0.f, 0.f, 0.f, 0.f, 0.f, 0.f, 0.f};
#pragma unroll
  for (int i = 0; i < 4; ++i) mac(wd[i], (unsigned)__builtin_amdgcn_ds_bpermute((16 * i + rho) * 4, hp), y);
  const f2 ys = reduce8(y);
  if ((rho & 1) == 0) put2(rs, a_py, asu(ys.x), asu(ys.y), e);
  STAMP(5);
  FLUSH(5, 5);
  if (s >= 8) return;
  __builtin_amdgcn_s_sleep(DY);
  v4u yv[2];
  gather<2, false>(rs, oy, e, yv);
  const float yc = rbf(reduce_quads(asf(yv[0].x) + (two ? asf(yv[1].x) : 0.f), asf(yv[0].y) + (two ? asf(yv[1].y) : 0.f)));
  if ((lane & 28) == 0) *(LAS f2*)(L + L_B + ((lane >> 5) * 4 + (lane & 3)) * 8) = f2{rbf(xn1 + rbf(yc * sg)), x1};
  asm volatile("s_waitcnt lgkmcnt(0)" ::: "memory");
  STAMP(7);
  FLUSH(7, 7);
}

extern "C" __device__ __attribute__((used, noinline)) void END_MARK() {}

extern "C" void kernel_launch(void* const* d_in, const int* in_sizes, int n_in, void* d_out, int out_size, void* d_ws, size_t ws_size,
                              hipStream_t stream) {
  (void)in_sizes; (void)n_in; (void)out_size;
  Params p;
  p.x = (const unsigned short*)d_in[0];
  p.k = (const unsigned short*)d_in[1];
  p.v = (const unsigned short*)d_in[2];
  p.wq = (const unsigned short*)d_in[3];
  p.wo = (const unsigned short*)d_in[4];
  p.wr = (const unsigned short*)d_in[6];
  p.wg = (const unsigned short*)d_in[7];
  p.wu = (const unsigned short*)d_in[8];
  p.wd = (const unsigned short*)d_in[9];
  p.out = (unsigned*)d_out;
  p.ws = (unsigned char*)d_ws + (WSTAIL ? (ws_size - WS_SPAN) / 4096 * 4096 : 0);
  hipLaunchKernelGGL(KERNEL_NAME, dim3(NWG), dim3(NTHR), 0, stream, p);
}
```

```cpp
#include <hip/hip_runtime.h>
#include <stddef.h>

#define GAS __attribute__((address_space(1)))
#define LAS __attribute__((address_space(3)))
typedef unsigned v4u __attribute__((ext_vector_type(4)));
typedef unsigned v2u __attribute__((ext_vector_type(2)));
typedef unsigned v16u __attribute__((ext_vector_type(16)));
typedef float f4 __attribute__((ext_vector_type(4)));
typedef float f2 __attribute__((ext_vector_type(2)));
typedef short s16x4 __attribute__((ext_vector_type(4)));
typedef __bf16 bf16x8 __attribute__((ext_vector_type(8)));
typedef __bf16 bf16x2 __attribute__((ext_vector_type(2)));

namespace {

constexpr int D = 512, DH = 128, SK = 65536, FF = 1792;
constexpr int NWG = 256, NTHR = 448;
constexpr int KEYS_PER_WG = SK / 32;
constexpr int NSTEP = KEYS_PER_WG / 32;
constexpr int RING = 8;
constexpr int PEND = 4 * (NSTEP - RING / 2);
constexpr int NFFN = FF / 64;
constexpr float SCALE = 0.08838834764831845f, LOG2E = 1.4426950408889634f, FMIN = -3.4028234664e38f;
constexpr int QD = 26, WD = 14, PACE0 = 4, GAIN = 24, UNOM = 650, PSTEP = 50, D1 = 22, D2 = 14, DY = 16;
constexpr unsigned SPIN_CAP = 1u << 16;
constexpr int TAU16 = 215;

constexpr int ROW = 4096 + 128;
constexpr int WS_EPOCH = 0;
constexpr int WS_Q = 4096;
constexpr int WS_GU = WS_Q + 8 * 4 * 4096;
constexpr int WS_Y = WS_GU + NFFN * 8192;
constexpr int WS_O = WS_Y + NFFN * ROW;
constexpr int WS_A = WS_O + 256 * ROW;
constexpr int WS_PL = WS_A + 256 * ROW;
constexpr int WS_END = WS_PL + 32 * 128;
constexpr size_t WS_SPAN = 4u << 20;
static_assert(WS_END <= WS_SPAN, "workspace map");

constexpr int L_Q = 0, L_E = 1024, L_FLAG = 1032, L_CNT = 1036, L_PROG = 1040, L_LAG = 1056, L_REF = 1072, L_CNTQ = 1080, L_B = 1088, L_FIN = 1152;
constexpr int L_ML = 1280;
constexpr int REC = 4 * 132 * 4, L_REC = 1536;
constexpr int L_WO = 16384, L_WG = 32768, L_WU = 40960, L_WD = 49152;
constexpr int L_WQ = 74752, L_X = 91136;
constexpr int L_S = 58368;
constexpr int L_SINK = 57344;
constexpr int LDS_BYTES = 92 * 1024;
static_assert(L_FLAG + 56 == L_B && L_FIN + 128 <= L_ML && L_ML + 128 <= L_REC && L_REC + 4 * REC <= L_WO && L_WD + 8192 <= L_SINK && L_S + 16384 <= L_WQ && L_X + 2048 <= LDS_BYTES, "LDS map");

struct Params {
  const unsigned short *x, *k, *v, *wq, *wo, *wr, *wg, *wu, *wd;
  unsigned* out;
  unsigned char* ws;
};
static_assert(sizeof(Params) == 88 && offsetof(Params, wq) == 0x18 && offsetof(Params, wd) == 0x40 && offsetof(Params, ws) == 0x50, "entry code");

__device__ __forceinline__ float asf(unsigned u) { return __builtin_bit_cast(float, u); }
__device__ __forceinline__ unsigned asu(float f) { return __builtin_bit_cast(unsigned, f); }
__device__ __forceinline__ float lo(unsigned u) { return asf(u << 16); }
__device__ __forceinline__ float hi(unsigned u) { return asf(u & 0xffff0000u); }
__device__ __forceinline__ unsigned pk(float a, float b) {
  bf16x2 r = {(__bf16)a, (__bf16)b};
  return __builtin_bit_cast(unsigned, r);
}
__device__ __forceinline__ float rbf(float a) { return lo(pk(a, a)); }
__device__ __forceinline__ float rcp(float a) { return __builtin_amdgcn_rcpf(a); }
__device__ __forceinline__ float ex2(float a) { return __builtin_amdgcn_exp2f(a); }
__device__ __forceinline__ unsigned xor3(unsigned a, unsigned b, unsigned c) { return __builtin_amdgcn_bitop3_b32(a, b, c, 0x96); }
__device__ __forceinline__ unsigned or3(unsigned a, unsigned b, unsigned c) { return __builtin_amdgcn_bitop3_b32(a, b, c, 0xFE); }
__device__ __forceinline__ unsigned uni(unsigned a) { return (unsigned)__builtin_amdgcn_readfirstlane((int)a); }
__device__ __forceinline__ unsigned clock32() { return (unsigned)__builtin_amdgcn_s_memrealtime(); }
__device__ __forceinline__ float dot2(unsigned a, unsigned b, float c) {
  return __builtin_amdgcn_fdot2_f32_bf16(__builtin_bit_cast(bf16x2, a), __builtin_bit_cast(bf16x2, b), c, false);
}
template <class T> __device__ __forceinline__ void pin(T& x) { asm volatile("" : "+v"(x)); }
template <int CTRL> __device__ __forceinline__ float dpp(float x) {
  return asf(__builtin_amdgcn_mov_dpp(__builtin_bit_cast(int, x), CTRL, 0xf, 0xf, true));
}
constexpr int X1 = 0xB1, X2 = 0x4E, X7 = 0x141, R4 = 0x124, R8 = 0x128;
__device__ __forceinline__ float x32_tr(float a, float b) {
  auto s = __builtin_amdgcn_permlane32_swap(asu(a), asu(b), false, false);
  return asf(s[0]) + asf(s[1]);
}
__device__ __forceinline__ float x16_tr(float a, float b) {
  auto s = __builtin_amdgcn_permlane16_swap(asu(a), asu(b), false, false);
  return asf(s[0]) + asf(s[1]);
}
__device__ __forceinline__ float rows_add(float x) { x = x16_tr(x, x); return x32_tr(x, x); }
__device__ __forceinline__ float rows_max(float x) {
  auto s = __builtin_amdgcn_permlane16_swap(asu(x), asu(x), false, false);
  x = fmaxf(asf(s[0]), asf(s[1]));
  auto t = __builtin_amdgcn_permlane32_swap(asu(x), asu(x), false, false);
  return fmaxf(asf(t[0]), asf(t[1]));
}
__device__ __forceinline__ float wave_sum(float x) {
  x += dpp<X1>(x); x += dpp<X2>(x); x += dpp<X7>(x); x += dpp<R8>(x);
  return rows_add(x);
}
__device__ __forceinline__ void lds_barrier() {
  asm volatile("s_waitcnt lgkmcnt(0)" ::: "memory");
  __builtin_amdgcn_s_barrier();
  asm volatile("" ::: "memory");
}
template <int N> __device__ __forceinline__ void pace() {
  __builtin_amdgcn_sched_barrier(0);
  asm volatile("s_waitcnt vmcnt(%0)" :: "i"(N) : "memory");
  __builtin_amdgcn_sched_barrier(0);
}
__device__ __forceinline__ void pace_lvl(unsigned lvl) {
  __builtin_amdgcn_sched_barrier(0);
  asm volatile("s_cmp_gt_u32 %0, 2\n\ts_cbranch_scc1 3f\n\ts_cmp_eq_u32 %0, 2\n\ts_cbranch_scc1 2f\n\ts_cmp_eq_u32 %0, 1\n\ts_cbranch_scc1 1f\n\t"
               "s_waitcnt vmcnt(2)\n1:\n\ts_waitcnt vmcnt(3)\n2:\n\ts_waitcnt vmcnt(4)\n\ts_branch 9f\n"
               "3:\n\ts_cmp_gt_u32 %0, 5\n\ts_cbranch_scc1 9f\n\ts_cmp_eq_u32 %0, 5\n\ts_cbranch_scc1 5f\n\ts_cmp_eq_u32 %0, 4\n\ts_cbranch_scc1 4f\n\t"
               "s_waitcnt vmcnt(5)\n4:\n\ts_waitcnt vmcnt(6)\n5:\n\ts_waitcnt vmcnt(7)\n9:"
               :: "s"(lvl) : "scc");
  __builtin_amdgcn_sched_barrier(0);
}
constexpr int SC1 = 16, NT = 2;
__device__ __forceinline__ void put2(__amdgpu_buffer_rsrc_t rs, int off, unsigned a, unsigned b, v2u e) {
  __builtin_amdgcn_raw_buffer_store_b128(v4u{a, b, a ^ e.x, b ^ e.y}, rs, off, 0, SC1);
}
__device__ __forceinline__ void put3(__amdgpu_buffer_rsrc_t rs, int off, unsigned a, unsigned b, unsigned c, v2u e) {
  __builtin_amdgcn_raw_buffer_store_b128(v4u{a, b, c, xor3(a, b, c) ^ e.x}, rs, off, 0, SC1);
}
template <int N, bool THREE> __device__ __forceinline__ void gather(__amdgpu_buffer_rsrc_t rs, const int (&off)[N], v2u e, v4u (&g)[N]) {
  unsigned spin = SPIN_CAP;
#pragma unroll 1
  for (;;) {
    asm volatile("s_sub_u32 %0, %0, 1" : "+s"(spin) :: "memory", "scc");
#pragma unroll
    for (int i = 0; i < N; ++i) g[i] = __builtin_amdgcn_raw_buffer_load_b128(rs, off[i], 0, SC1);
    unsigned bad = 0;
#pragma unroll
    for (int i = 0; i < N; ++i) {
      if (THREE) bad |= xor3(xor3(g[i].x, g[i].y, g[i].z), g[i].w, e.x);
      else bad = or3(xor3(g[i].x, g[i].z, e.x), xor3(g[i].y, g[i].w, e.y), bad);
    }
    if (__builtin_amdgcn_ballot_w64(bad != 0u) == 0ull) break;
    if (spin == 0u) break;
  }
}
template <unsigned V> __device__ __forceinline__ void wait_word(LAS unsigned char* w) {
#pragma unroll 1
  for (unsigned spin = 0; spin < (1u << 21); ++spin) {
    if (uni(*(volatile LAS unsigned*)w) >= V) break;
    __builtin_amdgcn_s_sleep(1);
  }
  asm volatile("" ::: "memory");
}
__device__ __forceinline__ unsigned progress(LAS unsigned char* L) {
  const v4u pr = *(volatile LAS v4u*)(L + L_PROG);
  return uni(pr.x + pr.y + pr.z + pr.w);
}
__device__ __forceinline__ void dma(const void* src, unsigned dst) {
  unsigned keep;
  asm volatile("s_mov_b32 %0, m0\n\ts_mov_b32 m0, %2\n\ts_nop 0\n\tglobal_load_lds_dwordx4 %1, off nt\n\ts_mov_b32 m0, %0"
               : "=&s"(keep) : "v"(src), "s"(dst) : "memory");
}
__device__ __forceinline__ void ln_stats(v4u xa, float& mean, float& rden) {
  const unsigned one = 0x3f803f80u;
  const float mu = wave_sum(dot2(xa.x, one, dot2(xa.y, one, 0.f)) + dot2(xa.z, one, dot2(xa.w, one, 0.f))) * (1.0f / D);
  float q0 = 0.f, q1 = 0.f;
#pragma unroll
  for (int j = 0; j < 4; ++j) { const float a = lo(xa[j]) - mu, b = hi(xa[j]) - mu; q0 = fmaf(a, a, q0); q1 = fmaf(b, b, q1); }
  const float var = rbf(wave_sum(q0 + q1) * (1.0f / D));
  mean = rbf(mu);
  rden = rcp(rbf(__builtin_amdgcn_sqrtf(rbf(var + 1e-5f))));
}
__device__ __forceinline__ unsigned ln_pair(unsigned xx, float mean, float rden) {
  const unsigned d = pk(lo(xx) - mean, hi(xx) - mean);
  return pk(lo(d) * rden, hi(d) * rden);
}
__device__ __forceinline__ v4u ln_apply(v4u xa, float mean, float rden) {
  return v4u{ln_pair(xa.x, mean, rden), ln_pair(xa.y, mean, rden), ln_pair(xa.z, mean, rden), ln_pair(xa.w, mean, rden)};
}
struct Pairs { unsigned c[8]; };
__device__ __forceinline__ Pairs pair_rows(v4u w0, v4u w1) {
  Pairs r;
#pragma unroll
  for (int k = 0; k < 4; ++k) {
    r.c[2 * k] = __builtin_amdgcn_perm(w1[k], w0[k], 0x05040100u);
    r.c[2 * k + 1] = __builtin_amdgcn_perm(w1[k], w0[k], 0x07060302u);
  }
  return r;
}
__device__ __forceinline__ void mac(const Pairs& w, unsigned x, float (&acc)[8]) {
#pragma unroll
  for (int k = 0; k < 8; ++k) acc[k] = dot2(w.c[k], x, acc[k]);
}
__device__ __forceinline__ f2 reduce8(const float (&acc)[8]) {
  const float t0 = x32_tr(acc[0], acc[4]), t1 = x32_tr(acc[1], acc[5]), t2 = x32_tr(acc[2], acc[6]), t3 = x32_tr(acc[3], acc[7]);
  const float u0 = x16_tr(t0, t2), u1 = x16_tr(t1, t3);
  return f2{u0 + dpp<R8>(u0), u1 + dpp<R8>(u1)};
}
__device__ __forceinline__ float reduce_quads(float a, float b) {
  float t = x32_tr(a, b);
  t = x16_tr(t, t);
  t += dpp<R4>(t);
  return t + dpp<R8>(t);
}

__device__ __forceinline__ unsigned scores(v4u ka, v4u kb, bf16x8 qf) {
  const f4 z = {0.f, 0.f, 0.f, 0.f};
  const f4 da = __builtin_amdgcn_mfma_f32_16x16x32_bf16(__builtin_bit_cast(bf16x8, ka), qf, z, 0, 0, 0);
  const f4 db = __builtin_amdgcn_mfma_f32_16x16x32_bf16(__builtin_bit_cast(bf16x8, kb), qf, z, 0, 0, 0);
  const float sa = (dpp<0x00>(da[0]) + dpp<0x55>(da[1])) + (dpp<0xAA>(da[2]) + dpp<0xFF>(da[3]));
  const float sb = (dpp<0x00>(db[0]) + dpp<0x55>(db[1])) + (dpp<0xAA>(db[2]) + dpp<0xFF>(db[3]));
  const unsigned u = pk(sa, sb);
  return pk(lo(u) * SCALE, hi(u) * SCALE);
}
__device__ __forceinline__ void pv(v4u va, v4u vb, unsigned pp, v4u sel, f4& o0, f4& o1) {
  const v2u ba = {__builtin_amdgcn_perm(pp, pp, sel.x), __builtin_amdgcn_perm(pp, pp, sel.y)};
  const v2u bb = {__builtin_amdgcn_perm(pp, pp, sel.z), __builtin_amdgcn_perm(pp, pp, sel.w)};
  o0 = __builtin_amdgcn_mfma_f32_16x16x16bf16_1k(__builtin_bit_cast(s16x4, v2u{va.x, va.y}), __builtin_bit_cast(s16x4, ba), o0, 0, 0, 0);
  o1 = __builtin_amdgcn_mfma_f32_16x16x16bf16_1k(__builtin_bit_cast(s16x4, v2u{va.z, va.w}), __builtin_bit_cast(s16x4, ba), o1, 0, 0, 0);
  o0 = __builtin_amdgcn_mfma_f32_16x16x16bf16_1k(__builtin_bit_cast(s16x4, v2u{vb.x, vb.y}), __builtin_bit_cast(s16x4, bb), o0, 0, 0, 0);
  o1 = __builtin_amdgcn_mfma_f32_16x16x16bf16_1k(__builtin_bit_cast(s16x4, v2u{vb.z, vb.w}), __builtin_bit_cast(s16x4, bb), o1, 0, 0, 0);
}

}

extern "C" __global__ void __launch_bounds__(NTHR, 2) fused_decode_kernel(Params) {
  __shared__ __attribute__((aligned(16))) unsigned char smem[LDS_BYTES];
  LAS unsigned char* L = (LAS unsigned char*)smem;
  v16u arg0; v4u arg1; v2u arg2;
  int wave;
  unsigned long long code;
  unsigned code_last;
  {
    unsigned long long wq, px;
    unsigned t0, t1, m0s, v0, v1;
    asm volatile("s_load_dwordx16 %0, %13, 0x0\n\ts_load_dwordx4 %1, %13, 0x40\n\ts_load_dwordx2 %2, %13, 0x50\n\ts_load_dwordx2 %4, %13, 0x18\n\t"
                 "s_load_dwordx2 %12, %13, 0x0\n\tv_readfirstlane_b32 %5, %14\n\tv_and_b32 %9, 63, %14\n\t"
                 ".p2align 4\n\tv_lshlrev_b32 %9, 4, %9\n\ts_lshr_b32 %5, %5, 6\n\ts_movk_i32 %7, 0x7ff0\n\t"
                 "s_getpc_b64 %3\n\ts_mov_b32 %11, fused_decode_code_end@rel32@lo-12\n\ts_and_b32 %11, %11, -16\n\t"
                 "s_mov_b32 %8, m0\n\ts_lshr_b32 %6, %15, 3\n\t"
                 "s_cmp_gt_u32 %11, %7\n\ts_cselect_b32 %11, 0, %11\n\ts_cmp_lg_u32 %5, 5\n\ts_cbranch_scc1 1f\n\t"
                 "v_lshl_or_b32 %10, %6, 10, %9\n\ts_mov_b32 m0, %16\n\tv_min_u32 %10, %11, %10\n\tglobal_load_lds_dwordx4 %10, %3\n"
                 "1:\n\ts_cmp_gt_u32 %5, 3\n\ts_cbranch_scc1 2f\n\t"
                 "s_lshl_b32 %7, %5, 12\n\ts_add_u32 m0, %7, %17\n\t"
                 "s_and_b32 %7, %6, 3\n\ts_lshl_b32 %7, %7, 5\n\ts_add_u32 %7, %7, %5\n\ts_lshl_b32 %7, %7, 15\n\t"
                 "s_lshr_b32 %6, %6, 2\n\ts_lshl_b32 %6, %6, 7\n\ts_add_u32 %7, %7, %6\n\t"
                 "s_and_b32 %6, %15, 7\n\ts_xor_b32 %6, %6, 1\n\ts_lshl_b32 %6, %6, 10\n\ts_add_u32 %7, %7, %6\n\t"
                 "v_lshrrev_b32 %10, 7, %9\n\tv_and_b32 %9, 0x70, %9\n\tv_lshl_or_b32 %10, %10, 17, %9\n\tv_add_u32 %10, %7, %10\n\t"
                 "s_waitcnt lgkmcnt(0)\n\t"
                 "global_load_lds_dwordx4 %10, %4 nt\n\ts_add_u32 m0, m0, 0x400\n\tv_add_u32 %10, 0x2000, %10\n\t"
                 "global_load_lds_dwordx4 %10, %4 nt\n\ts_add_u32 m0, m0, 0x400\n\tv_add_u32 %10, 0x2000, %10\n\t"
                 "global_load_lds_dwordx4 %10, %4 nt\n\ts_add_u32 m0, m0, 0x400\n\tv_add_u32 %10, 0x2000, %10\n\t"
                 "global_load_lds_dwordx4 %10, %4 nt\n\ts_branch 3f\n"
                 "2:\n\ts_cmp_lg_u32 %5, 6\n\ts_cbranch_scc1 4f\n\t"
                 "v_min_u32 %10, 0xd0, %9\n\tv_lshrrev_b32 %10, 2, %10\n\tv_mov_b32 %9, 0\n\tds_write_b32 %10, %9 offset:%19\n"
                 "4:\n\ts_mov_b32 m0, %18\n\ts_cmp_lg_u32 %5, 4\n\ts_waitcnt lgkmcnt(0)\n\ts_cbranch_scc1 3f\n\t"
                 "global_load_lds_dwordx4 %9, %12\n\ts_add_u32 m0, m0, 0x400\n\ts_nop 0\n\tglobal_load_lds_dwordx4 %9, %2 sc1\n"
                 "3:\n\ts_mov_b32 m0, %8\n\ts_barrier"
                 : "=&s"(arg0), "=&s"(arg1), "=&s"(arg2), "=&s"(code), "=&s"(wq), "=&s"(wave), "=&s"(t0), "=&s"(t1), "=&s"(m0s), "=&v"(v0), "=&v"(v1),
                   "=&s"(code_last), "=&s"(px)
                 : "s"(__builtin_amdgcn_kernarg_segment_ptr()), "v"(threadIdx.x), "s"(blockIdx.x), "i"(L_SINK), "i"(L_WQ), "i"(L_X), "i"(L_FLAG)
                 : "scc", "memory");
  }
  const int lane = threadIdx.x & 63;
  const int b = blockIdx.x, g = b & 7, s = b >> 3;
  Params p;
  {
    auto ptr = [](unsigned l, unsigned h) { return (const unsigned short*)(const GAS unsigned short*)(((unsigned long long)h << 32) | l); };
    p.x = ptr(arg0[0], arg0[1]); p.k = ptr(arg0[2], arg0[3]); p.v = ptr(arg0[4], arg0[5]); p.wq = ptr(arg0[6], arg0[7]);
    p.wo = ptr(arg0[8], arg0[9]); p.wr = ptr(arg0[10], arg0[11]); p.wg = ptr(arg0[12], arg0[13]); p.wu = ptr(arg0[14], arg0[15]);
    p.wd = ptr(arg1[0], arg1[1]); p.out = (unsigned*)ptr(arg1[2], arg1[3]); p.ws = (unsigned char*)ptr(arg2[0], arg2[1]);
  }
  const int gx = g ^ 1;
  const __amdgpu_buffer_rsrc_t rs = __builtin_amdgcn_make_buffer_rsrc((void*)p.ws, 0, WS_END, 0x00020000);

  if (__builtin_expect(wave < 4, 1)) {
    const size_t base = ((size_t)g * SK + (size_t)s * KEYS_PER_WG) * DH;
    const __amdgpu_buffer_rsrc_t rk = __builtin_amdgcn_make_buffer_rsrc((void*)(p.k + base), 0, KEYS_PER_WG * DH * 2, 0x00020000);
    const __amdgpu_buffer_rsrc_t rv = __builtin_amdgcn_make_buffer_rsrc((void*)(p.v + base), 0, KEYS_PER_WG * DH * 2, 0x00020000);
    const int h = (lane >> 2) & 3, c = lane & 3;
    const int frag = h * 256 + (lane >> 4) * 64 + c * 16;
    int ko = wave * 2048 + frag;
    const int vo = wave * 2048 + lane * 16;
    constexpr int STRIDE = 32 * DH * 2;
    const unsigned lb = (unsigned)(size_t)L;
    const int row = 8 * (lane >> 3) + 2 * wave;
    const unsigned short* wg = p.wg + (size_t)(g * 64 + row) * FF + s * 64 + (lane & 7) * 8;
    const unsigned short* wu = p.wu + (size_t)(g * 64 + row) * FF + s * 64 + (lane & 7) * 8;
    const unsigned short* wd = p.wd + (size_t)(s * 64 + row) * D + g * 64 + (lane & 7) * 8;
    const unsigned short* wo = p.wo + (size_t)(gx * 512 + s * 16 + 4 * wave) * D + 8 * lane;
    v4u ra[RING], rb[RING];
    __builtin_amdgcn_s_sleep(WD);
    dma(wo, lb + L_WO + 4 * wave * 1024);
    dma(wo + D, lb + L_WO + (4 * wave + 1) * 1024);
    pace<2>();
    if (lane == 0) __hip_atomic_fetch_add((LAS unsigned*)(L + L_CNTQ), 1u, __ATOMIC_RELAXED, __HIP_MEMORY_SCOPE_WORKGROUP);
    dma(wo + 2 * D, lb + L_WO + (4 * wave + 2) * 1024);
    dma(wo + 3 * D, lb + L_WO + (4 * wave + 3) * 1024);
    if (s < NFFN) {
      dma(wg, lb + L_WG + 2 * wave * 1024);
      dma(wu, lb + L_WU + 2 * wave * 1024);
      pace<PACE0>();
      dma(wg + FF, lb + L_WG + (2 * wave + 1) * 1024);
      dma(wu + FF, lb + L_WU + (2 * wave + 1) * 1024);
      pace<PACE0>();
      dma(wd, lb + L_WD + 2 * wave * 1024);
      dma(wd + D, lb + L_WD + (2 * wave + 1) * 1024);
    }
#pragma unroll
    for (int r = 0; r < RING; ++r) {
      pace<PACE0>();
      if (r == (PACE0 + 1) / 2 && lane == 0)
        __hip_atomic_fetch_add((LAS unsigned*)(L + L_CNT), 1u, __ATOMIC_RELAXED, __HIP_MEMORY_SCOPE_WORKGROUP);
      ra[r] = __builtin_amdgcn_raw_buffer_load_b128(rk, ko, 0, NT);
      rb[r] = __builtin_amdgcn_raw_buffer_load_b128(rk, ko + 1024, 0, NT);
      ko += STRIDE;
    }
    __builtin_amdgcn_sched_barrier(0);
    const unsigned z = 0x0c0c0c0cu;
    const v4u sel = {c == 0 ? 0x0c0c0100u : c == 1 ? 0x01000c0cu : z, c == 2 ? 0x0c0c0100u : c == 3 ? 0x01000c0cu : z,
                     c == 0 ? 0x0c0c0302u : c == 1 ? 0x03020c0cu : z, c == 2 ? 0x0c0c0302u : c == 3 ? 0x03020c0cu : z};
    int a_ml = L_ML + h * 32 + wave * 8, a_ow = L_REC + wave * REC + (h * 132 + (lane >> 4) * 32 + c) * 4, a_or = L_REC + (wave * 132 + 2 * lane) * 4;
    int a_po = WS_O + (g * 32 + s) * ROW + threadIdx.x * 16;
    int a_s = L_S + wave * 4096 + (lane >> 2) * 4;
    pin(a_ml); pin(a_ow); pin(a_or); pin(a_po); pin(a_s);
    wait_word<1>(L + L_FLAG);
    const bf16x8 qf = __builtin_bit_cast(bf16x8, *(const LAS v4u*)(L + L_Q + frag));
    v2u e = *(const LAS v2u*)(L + L_E);
    pin(e);
    auto depth = [&](int n2) {
      const v2u ref = *(volatile LAS v2u*)(L + L_REF);
      const unsigned lag = clock32() - (n2 * (2 * TAU16) >> 4);
      *(volatile LAS unsigned*)(L + L_PROG + 4 * wave) = (unsigned)(n2 >> 1);
      *(volatile LAS unsigned*)(L + L_LAG + 4 * wave) = lag;
      int lv = (UNOM - (int)((uni(ref.x) - lag) & uni(ref.y)) * GAIN) >> 8;
      asm("s_max_i32 %0, %0, 0\n\ts_min_i32 %0, %0, 5" : "+s"(lv) :: "scc");
      return lv;
    };
    float m = FMIN;
    int lv = 2;
#pragma unroll 1
    for (int i = 0; i < NSTEP; i += RING) {
      const bool last = i == NSTEP - RING;
      const __amdgpu_buffer_rsrc_t rn = __builtin_amdgcn_make_buffer_rsrc((void*)((last ? p.v : p.k) + base), 0, KEYS_PER_WG * DH * 2, 0x00020000);
      if (last) ko = vo;
#pragma unroll
      for (int r = 0; r < RING; ++r) {
        if ((r & 1) == 0) lv = depth(i + r);
        const unsigned u = scores(ra[r], rb[r], qf);
        pace_lvl(lv);
        ra[r] = __builtin_amdgcn_raw_buffer_load_b128(rn, ko, 0, NT);
        rb[r] = __builtin_amdgcn_raw_buffer_load_b128(rn, ko + 1024, 0, NT);
        ko += STRIDE;
        *(LAS unsigned*)(L + a_s + r * 64) = u;
        m = fmaxf(m, fmaxf(lo(u), hi(u)));
        __builtin_amdgcn_sched_barrier(0);
      }
      a_s += RING * 64;
    }
    m = rows_max(m);
    a_s -= NSTEP * 64;
    f4 o0 = {0.f, 0.f, 0.f, 0.f}, o1 = o0;
    float l = 0.f;
#pragma unroll 1
    for (int i = NSTEP - RING; i >= 0; i -= RING) {
      unsigned pp[RING];
#pragma unroll
      for (int r = 0; r < RING; ++r) {
        const unsigned u = *(const LAS unsigned*)(L + a_s + r * 64);
        const float pa = ex2((lo(u) - m) * LOG2E), pb = ex2((hi(u) - m) * LOG2E);
        l += pa + pb;
        pp[r] = pk(pa, pb);
      }
      *(volatile LAS f2*)(L + a_ml) = f2{m, rows_add(l)};
#pragma unroll
      for (int r = 0; r < RING; ++r) pin(pp[r]);
#pragma unroll
      for (int r = 0; r < RING; ++r) {
        if ((r & 1) == 0) { lv = depth(2 * NSTEP - RING + r - i); lv = i ? lv : 6; }
        __builtin_amdgcn_sched_barrier(0);
        pv(ra[r], rb[r], pp[r], sel, o0, o1);
        pin(o0); pin(o1);
        pace_lvl(lv);
        ra[r] = __builtin_amdgcn_raw_buffer_load_b128(rv, ko, 0, NT);
        rb[r] = __builtin_amdgcn_raw_buffer_load_b128(rv, ko + 1024, 0, NT);
        ko += STRIDE;
        __builtin_amdgcn_sched_barrier(0);
      }
      a_s += RING * 64;
    }
#pragma unroll
    for (int i = 0; i < 4; ++i) { *(LAS float*)(L + a_ow + 32 * i) = o0[i]; *(LAS float*)(L + a_ow + 32 * i + 16) = o1[i]; }
    lds_barrier();
    {
      const f4 ml0 = *(const LAS f4*)(L + L_ML + wave * 32), ml1 = *(const LAS f4*)(L + L_ML + wave * 32 + 16);
      const f2 t0 = *(const LAS f2*)(L + a_or), t1 = *(const LAS f2*)(L + a_or + REC), t2 = *(const LAS f2*)(L + a_or + 2 * REC), t3 = *(const LAS f2*)(L + a_or + 3 * REC);
      __builtin_amdgcn_sched_barrier(0);
      const float M = fmaxf(fmaxf(ml0.x, ml0.z), fmaxf(ml1.x, ml1.z));
      const float f0 = ex2((ml0.x - M) * LOG2E), f1 = ex2((ml0.z - M) * LOG2E), f2_ = ex2((ml1.x - M) * LOG2E), f3 = ex2((ml1.z - M) * LOG2E);
      const f2 o = (f0 * t0 + f1 * t1) + (f2_ * t2 + f3 * t3);
      const float ls = fmaf(f0, ml0.y, f1 * ml0.w) + fmaf(f2_, ml1.y, f3 * ml1.w);
      put3(rs, a_po, asu(o.x), asu(o.y), asu((lane & 1) ? ls : M), e);
    }
    if (s >= 8) return;
    const int sl = 64 * (s >> 1) + 8 * g + 4 * (s & 1) + c;
    int off[4];
#pragma unroll
    for (int i = 0; i < 4; ++i) { off[i] = WS_A + (64 * wave + 16 * i + (lane >> 2)) * ROW + sl * 16; pin(off[i]); }
    GAS unsigned short* po = (GAS unsigned short*)p.out + 2 * (4 * (sl & 63) + (sl >> 6)) + ((lane >> 2) & 1);
    int a_fw = L_FIN + wave * 32 + (lane >> 5) * 16 + c * 4;
    pin(po); pin(a_fw);
    wait_word<2>(L + L_FLAG);
    __builtin_amdgcn_s_sleep(D2);
    v4u t[4];
    gather<4, false>(rs, off, e, t);
    const float av = reduce_quads((asf(t[0].x) + asf(t[1].x)) + (asf(t[2].x) + asf(t[3].x)), (asf(t[0].y) + asf(t[1].y)) + (asf(t[2].y) + asf(t[3].y)));
    if ((lane & 28) == 0) *(LAS float*)(L + a_fw) = av;
    lds_barrier();
    if (wave == 0 && lane < 8) {
      const LAS float* fin = (const LAS float*)(L + L_FIN) + lane;
      const f2 bx = *(const LAS f2*)(L + L_B + lane * 8);
      const float r = rbf((fin[0] + fin[8]) + (fin[16] + fin[24]));
      *po = (unsigned short)pk(rbf(bx.y + r) + bx.x, 0.f);
    }
    return;
  }

  const int rho = lane >> 3, c = lane & 7;
  if (__builtin_expect(wave == 4, 1)) {
    const int kq = s & 3, strip = s >> 2;
    const int r0 = kq * 128 + 16 * rho;
    pace<0>();
    const v4u xa = *(const LAS v4u*)(L + L_X + 16 * lane);
    const v4u xb0 = *(const LAS v4u*)(L + L_X + 2 * r0), xb1 = *(const LAS v4u*)(L + L_X + 2 * r0 + 16);
    const v2u eo = *(const LAS v2u*)(L + L_X + 1024 + WS_EPOCH);
    float mean, rden;
    ln_stats(xa, mean, rden);
    v4u n0 = ln_apply(xb0, mean, rden), n1 = ln_apply(xb1, mean, rden);
    v2u e = {uni(eo.x) + 1u, uni(eo.y) + 1u};
    if (e.x == 0u) e.x = 1u;
    if (e.y == 0u) e.y = 1u;
    pin(n0); pin(n1);
    wait_word<4>(L + L_CNTQ);
    float acc[8] = {0.f, 0.f, 0.f, 0.f, 0.f, 0.f, 0.f, 0.f};
#pragma unroll
    for (int i = 0; i < 8; ++i) {
      const LAS unsigned char* w = L + L_WQ + 2 * i * 1024 + lane * 16;
      mac(pair_rows(*(const LAS v4u*)w, *(const LAS v4u*)(w + 1024)), i < 4 ? n0[i & 3] : n1[i & 3], acc);
    }
    const f2 u = reduce8(acc);
    if ((rho & 1) == 0) put2(rs, WS_Q + (gx * 4 + kq) * 4096 + (strip * 32 + (lane >> 4) * 8 + c) * 16, asu(u.x), asu(u.y), e);
    int off[16];
#pragma unroll
    for (int i = 0; i < 16; ++i) off[i] = WS_Q + (g * 4 + (i & 3)) * 4096 + ((i >> 2) * 64 + lane) * 16;
    v4u q[16];
    __builtin_amdgcn_s_sleep(QD);
    gather<16, false>(rs, off, e, q);
#pragma unroll
    for (int i = 0; i < 4; ++i)
      *(LAS unsigned*)(L + L_Q + (i * 64 + (lane & 32) + 4 * c + (rho & 3)) * 4) =
          pk((asf(q[4 * i].x) + asf(q[4 * i + 1].x)) + (asf(q[4 * i + 2].x) + asf(q[4 * i + 3].x)),
             (asf(q[4 * i].y) + asf(q[4 * i + 1].y)) + (asf(q[4 * i + 2].y) + asf(q[4 * i + 3].y)));
    if (lane == 0) *(LAS v2u*)(L + L_E) = e;
    asm volatile("s_waitcnt lgkmcnt(0)" ::: "memory");
    if (lane == 0) *(volatile LAS unsigned*)(L + L_FLAG) = 1u;
    const bool rd = lane < 8 && lane != g;
    unsigned last = 0, t_ref = 0, ref = 0, t_first = 0, ref_first = 0, pub_at = 8, rd_at = 18;
    float drift = 0.f;
    int nref = 0;
#pragma unroll 1
    for (int it = 0; it < 16384 && last < PEND; ++it) {
      const bool exch = last >= rd_at;
      v4u t = {0u, 0u, 0u, 0u};
      asm volatile("" ::: "memory");
      if (exch && rd) t = __builtin_amdgcn_raw_buffer_load_b128(rs, WS_PL + (((s + 5 * nref + 3 * lane) & 24) + lane) * 128, 0, SC1);
      asm volatile("" : "+v"(t) :: "memory");
      const v4u pr = *(volatile LAS v4u*)(L + L_PROG), lg = *(volatile LAS v4u*)(L + L_LAG);
      const unsigned now = clock32();
      last = uni(pr.x + pr.y + pr.z + pr.w);
      const bool begun = uni(min(min(pr.x, pr.y), min(pr.z, pr.w))) != 0u;
      const unsigned lag = begun ? uni(lg.x + (unsigned)(((int)(lg.y - lg.x) + (int)(lg.z - lg.x) + (int)(lg.w - lg.x)) >> 2)) : now - (last * TAU16 >> 4);
      if (last >= pub_at) {
        if (lane == 0 && (s & 7) == 0) put2(rs, WS_PL + (s + g) * 128, lag, now, e);
        pub_at += PSTEP;
      }
      if (exch) {
        rd_at += PSTEP;
        const bool ok = rd && (t.x ^ t.z) == e.x && (t.y ^ t.w) == e.y;
        float sl = ok ? (float)(int)(t.x - lag) : 0.f, st = ok ? (float)(int)(t.y - now) : 0.f;
        sl += dpp<X1>(sl); st += dpp<X1>(st); sl += dpp<X2>(sl); st += dpp<X2>(st); sl += dpp<X7>(sl); st += dpp<X7>(st);
        const int n = __builtin_popcountll(__builtin_amdgcn_ballot_w64(ok));
        if (n >= 4) {
          const float w = rcp((float)(n + 1));
          ref = lag + uni((unsigned)(int)(sl * w));
          t_ref = now + uni((unsigned)(int)(st * w));
          if (nref == 0) { ref_first = ref; t_first = t_ref; }
          else drift = (float)(int)(ref - ref_first) * rcp((float)(int)(t_ref - t_first) + 200.f);
          ++nref;
        }
      }
      if (lane == 0 && begun) *(volatile LAS v2u*)(L + L_REF) = v2u{nref ? ref + (unsigned)(int)(drift * (float)(int)(now - t_ref)) : lag, ~0u};
      __builtin_amdgcn_s_sleep(5);
    }
    return;
  }

  if (wave == 5) {
    wait_word<4>(L + L_CNT);
    Pairs w[8];
#pragma unroll
    for (int j = 0; j < 8; ++j) {
      w[j] = pair_rows(*(const LAS v4u*)(L + L_WO + 2 * j * 1024 + lane * 16), *(const LAS v4u*)(L + L_WO + (2 * j + 1) * 1024 + lane * 16));
#pragma unroll
      for (int k = 0; k < 8; ++k) pin(w[j].c[k]);
    }
    int off[4], a_pa = WS_A + (g * 32 + s) * ROW + lane * 16;
#pragma unroll
    for (int i = 0; i < 4; ++i) { off[i] = WS_O + (gx * 32 + rho + 8 * i) * ROW + s * 128 + c * 16; pin(off[i]); }
    pin(a_pa);
    float acc[8] = {0.f, 0.f, 0.f, 0.f, 0.f, 0.f, 0.f, 0.f};
#pragma unroll
    for (int k = 0; k < 8; ++k) pin(acc[k]);
    lds_barrier();
    const v2u e = *(const LAS v2u*)(L + L_E);
    __builtin_amdgcn_s_sleep(D1);
    v4u t[4];
    gather<4, true>(rs, off, e, t);
    const float m0 = dpp<0xA0>(asf(t[0].z)), m1 = dpp<0xA0>(asf(t[1].z)), m2 = dpp<0xA0>(asf(t[2].z)), m3 = dpp<0xA0>(asf(t[3].z));
    float M = fmaxf(fmaxf(m0, m1), fmaxf(m2, m3));
    M = rows_max(fmaxf(M, dpp<R8>(M)));
    const float f0 = ex2((m0 - M) * LOG2E), f1 = ex2((m1 - M) * LOG2E), f2_ = ex2((m2 - M) * LOG2E), f3 = ex2((m3 - M) * LOG2E);
    float o0 = fmaf(f0, asf(t[0].x), f1 * asf(t[1].x)) + fmaf(f2_, asf(t[2].x), f3 * asf(t[3].x));
    float o1 = fmaf(f0, asf(t[0].y), f1 * asf(t[1].y)) + fmaf(f2_, asf(t[2].y), f3 * asf(t[3].y));
    float ls = dpp<0xF5>(fmaf(f0, asf(t[0].z), f1 * asf(t[1].z)) + fmaf(f2_, asf(t[2].z), f3 * asf(t[3].z)));
    o0 += dpp<R8>(o0); o1 += dpp<R8>(o1); ls += dpp<R8>(ls);
    o0 = rows_add(o0); o1 = rows_add(o1); ls = rcp(rows_add(ls));
    const unsigned op = pk(o0 * ls, o1 * ls);
    unsigned oj[8];
#pragma unroll
    for (int j = 0; j < 8; ++j) oj[j] = __builtin_amdgcn_readlane(op, j);
    __builtin_amdgcn_sched_barrier(0);
#pragma unroll
    for (int j = 0; j < 8; ++j) mac(w[j], oj[j], acc);
#pragma unroll
    for (int k = 0; k < 4; ++k) put2(rs, a_pa + k * 1024, asu(acc[2 * k]), asu(acc[2 * k + 1]), e);
    if (lane == 0) *(volatile LAS unsigned*)(L + L_FLAG) = 2u;
    return;
  }

  if (s >= NFFN) return;
  const int sl = 64 * ((s & 7) >> 1) + 8 * g + 4 * (s & 1) + (lane & 3), cp = 4 * (sl & 63) + (sl >> 6);
  const v4u xa = *(const GAS v4u*)(p.x + 8 * lane);
  const v4u wr = *(const GAS v4u*)(p.wr + 8 * lane);
  const unsigned xx = *(const GAS unsigned*)(p.x + 2 * cp);
  const v4u xb = *(const GAS v4u*)(p.x + g * 64 + 8 * rho);
  float mean, rden;
  ln_stats(xa, mean, rden);
  wait_word<4>(L + L_CNT);
  Pairs wd[4];
  unsigned gu0, gu1;
  {
    const v4u n = ln_apply(xb, mean, rden);
    float ga[8] = {0.f, 0.f, 0.f, 0.f, 0.f, 0.f, 0.f, 0.f}, ua[8] = {0.f, 0.f, 0.f, 0.f, 0.f, 0.f, 0.f, 0.f};
#pragma unroll
    for (int i = 0; i < 4; ++i) {
      const LAS unsigned char* w = L + 2 * i * 1024 + lane * 16;
      mac(pair_rows(*(const LAS v4u*)(w + L_WG), *(const LAS v4u*)(w + L_WG + 1024)), n[i], ga);
      mac(pair_rows(*(const LAS v4u*)(w + L_WU), *(const LAS v4u*)(w + L_WU + 1024)), n[i], ua);
      wd[i] = pair_rows(*(const LAS v4u*)(w + L_WD), *(const LAS v4u*)(w + L_WD + 1024));
#pragma unroll
      for (int k = 0; k < 8; ++k) pin(wd[i].c[k]);
    }
    const f2 gs = reduce8(ga), us = reduce8(ua);
    gu0 = asu((rho & 1) ? us.x : gs.x); gu1 = asu((rho & 1) ? us.y : gs.y);
  }
  const v4u nx = ln_apply(xa, mean, rden);
  const float r = rbf(wave_sum(dot2(nx.x, wr.x, dot2(nx.y, wr.y, 0.f)) + dot2(nx.z, wr.z, dot2(nx.w, wr.w, 0.f))));
  float sg = rbf(rcp(1.0f + ex2(-LOG2E * r)));
  const unsigned xnp = ln_pair(xx, mean, rden);
  float xn1 = lane < 32 ? lo(xnp) : hi(xnp), x1 = lane < 32 ? lo(xx) : hi(xx);
  int og[8], oy[2], a_py = WS_Y + s * ROW + (g * 4 + (lane >> 4)) * 128 + c * 16;
#pragma unroll
  for (int i = 0; i < 8; ++i) og[i] = WS_GU + (s * 8 + i) * 1024 + lane * 16;
  const bool two = (lane >> 2) + 16 < NFFN;
  oy[0] = WS_Y + ((cp >> 5) * 4 + (cp & 3)) * 128 + ((cp >> 2) & 7) * 16 + (lane >> 2) * ROW;
  oy[1] = oy[0] + (two ? 16 * ROW : 0);
  pin(oy[0]); pin(oy[1]);
  pin(gu0); pin(gu1); pin(sg); pin(xn1); pin(x1); pin(a_py);
  wait_word<1>(L + L_FLAG);
  const v2u e = *(const LAS v2u*)(L + L_E);
#pragma unroll 1
  for (int it = 0; it < 65536 && progress(L) < PEND - 40; ++it) __builtin_amdgcn_s_sleep(16);
  {
    const v4u t = *(const GAS v4u*)(code + min((unsigned)(s * 1024 + lane * 16), code_last));
    asm volatile("" :: "v"(t));
  }
#pragma unroll 1
  for (int it = 0; it < 65536 && progress(L) < PEND; ++it) __builtin_amdgcn_s_sleep(4);
  put2(rs, WS_GU + (s * 8 + g) * 1024 + lane * 16, gu0, gu1, e);
  lds_barrier();
  v4u gu[8];
  gather<8, false>(rs, og, e, gu);
  if (b == 0 && lane == 0) __builtin_amdgcn_raw_buffer_store_b64(e, rs, WS_EPOCH, 0, SC1);
  const unsigned ap = pk(((asf(gu[0].x) + asf(gu[1].x)) + (asf(gu[2].x) + asf(gu[3].x))) + ((asf(gu[4].x) + asf(gu[5].x)) + (asf(gu[6].x) + asf(gu[7].x))),
                         ((asf(gu[0].y) + asf(gu[1].y)) + (asf(gu[2].y) + asf(gu[3].y))) + ((asf(gu[4].y) + asf(gu[5].y)) + (asf(gu[6].y) + asf(gu[7].y))));
  const float a0 = lo(ap), a1 = hi(ap);
  const unsigned sp = pk(a0 * rcp(1.0f + ex2(-LOG2E * a0)), a1 * rcp(1.0f + ex2(-LOG2E * a1)));
  const int hp = (int)pk(lo(sp) * dpp<R8>(a0), hi(sp) * dpp<R8>(a1));
  float y[8] = {0.f, 0.f, 0.f, 0.f, 0.f, 0.f, 0.f, 0.f};
#pragma unroll
  for (int i = 0; i < 4; ++i) mac(wd[i], (unsigned)__builtin_amdgcn_ds_bpermute((16 * i + rho) * 4, hp), y);
  const f2 ys = reduce8(y);
  if ((rho & 1) == 0) put2(rs, a_py, asu(ys.x), asu(ys.y), e);
  if (s >= 8) return;
  __builtin_amdgcn_s_sleep(DY);
  v4u yv[2];
  gather<2, false>(rs, oy, e, yv);
  const float yc = rbf(reduce_quads(asf(yv[0].x) + (two ? asf(yv[1].x) : 0.f), asf(yv[0].y) + (two ? asf(yv[1].y) : 0.f)));
  if ((lane & 28) == 0) *(LAS f2*)(L + L_B + ((lane >> 5) * 4 + (lane & 3)) * 8) = f2{rbf(xn1 + rbf(yc * sg)), x1};
  asm volatile("s_waitcnt lgkmcnt(0)" ::: "memory");
}

extern "C" __device__ __attribute__((used, noinline)) void fused_decode_code_end() {}

extern "C" void kernel_launch(void* const* d_in, const int* in_sizes, int n_in, void* d_out, int out_size, void* d_ws, size_t ws_size,
                              hipStream_t stream) {
  (void)in_sizes; (void)n_in; (void)out_size;
  Params p;
  p.x = (const unsigned short*)d_in[0];
  p.k = (const unsigned short*)d_in[1];
  p.v = (const unsigned short*)d_in[2];
  p.wq = (const unsigned short*)d_in[3];
  p.wo = (const unsigned short*)d_in[4];
  p.wr = (const unsigned short*)d_in[6];
  p.wg = (const unsigned short*)d_in[7];
  p.wu = (const unsigned short*)d_in[8];
  p.wd = (const unsigned short*)d_in[9];
  p.out = (unsigned*)d_out;
  p.ws = (unsigned char*)d_ws + (ws_size - WS_SPAN) / 4096 * 4096;
  hipLaunchKernelGGL(fused_decode_kernel, dim3(NWG), dim3(NTHR), 0, stream, p);
}
```
